```python
import torch

def _softmax(x, axis=-1):
    return torch.softmax(x, dim=axis)

def _gelu(x):
    return torch.nn.functional.gelu(x, approximate='tanh')

def _silu(x):
    return torch.nn.functional.silu(x)

def _sigmoid(x):
    return torch.sigmoid(x)

def _layernorm(x, axis=-1, eps=1e-05):
    m = x.mean(dim=axis, keepdim=True)
    v = x.var(dim=axis, keepdim=True, unbiased=False)
    return (x - m) / torch.sqrt(v + eps)

def _scatter_add(tgt, idx, src, axis=0):
    return tgt.clone().index_add_(axis, idx, src)

def _segment_sum(src, seg, n):
    out = torch.zeros((n,) + src.shape[1:], dtype=src.dtype)
    return out.index_add_(0, seg, src)

def _pad(x, pw):
    flat = tuple((v for pair in reversed(pw) for v in pair))
    return torch.nn.functional.pad(x, flat)
kernel_name = 'recurrent_scan__memory_aug_recurrent_mem_tr__dense_activation_squared_relu__prompt'

def setup_inputs():

    _w = lambda a: (lambda t: t.to(torch.bfloat16) if t.is_floating_point() else t)(torch.as_tensor(a))

    def _A_setup():
        B, S, Dm = (1, 2048, 640)
        H, Dh = (32, 128)
        M, seg = (8, 32)
        seg = min(seg, S)
        S = S // seg * seg
        return {'x': _w((lambda *s: torch.randn(*s, dtype=torch.bfloat16) * 0.05)(B, S, Dm)), 'mem0': _w((lambda *s: torch.randn(*s, dtype=torch.bfloat16) * 0.05)(B, M, Dm)), 'Wq': _w((lambda *s: torch.randn(*s, dtype=torch.bfloat16) * 1.6)(Dm, H, Dh) * Dm ** (-0.5)), 'Wk': _w((lambda *s: torch.randn(*s, dtype=torch.bfloat16) * 1.6)(Dm, H, Dh) * Dm ** (-0.5)), 'Wv': _w((lambda *s: torch.randn(*s, dtype=torch.bfloat16) * 0.05)(Dm, H, Dh) * Dm ** (-0.5)), 'Wo': _w((lambda *s: torch.randn(*s, dtype=torch.bfloat16) * 0.05)(H, Dh, Dm) * (H * Dh) ** (-0.5)), 'seg_len': seg}
    _A_in = _A_setup()

    def _B_setup():
        B, S, D, F = (1, 2048, 640, 1728)
        return {'x': _w((lambda *s: torch.randn(*s, dtype=torch.bfloat16) * 0.05)(B, S, D)), 'w1': _w((lambda *s: torch.randn(*s, dtype=torch.bfloat16) * 0.05)(D, F)), 'b1': _w((lambda *s: torch.randn(*s, dtype=torch.bfloat16) * 0.4)(F)), 'w2': _w((lambda *s: torch.randn(*s, dtype=torch.bfloat16) * 0.05)(F, D)), 'b2': _w((lambda *s: torch.randn(*s, dtype=torch.bfloat16) * 3.2)(D))}
    _B_in = _B_setup()
    return {'A_x': _A_in['x'], 'A_mem0': _A_in['mem0'], 'A_Wq': _A_in['Wq'], 'A_Wk': _A_in['Wk'], 'A_Wv': _A_in['Wv'], 'A_Wo': _A_in['Wo'], 'A_seg_len': _A_in['seg_len'], 'B_x': _B_in['x'], 'B_w1': _B_in['w1'], 'B_b1': _B_in['b1'], 'B_w2': _B_in['w2'], 'B_b2': _B_in['b2']}

def reference(A_x, A_mem0, A_Wq, A_Wk, A_Wv, A_Wo, A_seg_len, B_x, B_w1, B_b1, B_w2, B_b2):

    def _A_ref(x, mem0, Wq, Wk, Wv, Wo, seg_len):
        B, S, Dm = x.shape
        H, Dh = (Wq.shape[1], Wq.shape[2])
        M = mem0.shape[1]
        scale = Dh ** (-0.5)
        n_seg = S // seg_len
        L = M + seg_len
        idx = torch.arange(L)
        mask = (idx[:, None] < M) | (idx[:, None] >= idx[None, :])
        mem = mem0
        outs = []
        for s in range(n_seg):
            xs = x[:, s * seg_len:(s + 1) * seg_len, :].clone()
            h = torch.concatenate([mem, xs], axis=1)
            q = torch.einsum('btd,dhe->bhte', h, Wq)
            k = torch.einsum('btd,dhe->bhte', h, Wk)
            v = torch.einsum('btd,dhe->bhte', h, Wv)
            scores = torch.einsum('bhqe,bhke->bhqk', q, k) * scale
            scores = torch.where(mask[None, None], scores, -1000000000.0)
            p = _softmax(scores, axis=-1)
            o = torch.einsum('bhqk,bhke->bhqe', p, v)
            o = torch.einsum('bhte,hed->btd', o, Wo)
            mem = o[:, :M, :].clone()
            outs.append(o[:, M:, :])
        return torch.concatenate(outs, axis=1)

    def _B_ref(x, w1, b1, w2, b2):
        h = torch.einsum('bsd,df->bsf', x, w1) + b1
        zero = torch.zeros_like(h)
        r = torch.where(h > zero, h, zero)
        h = r * r
        out = torch.einsum('bsf,fd->bsd', h, w2) + b2
        return out
    x = A_x
    xn = _layernorm(x, axis=-1)
    s = _A_ref(xn, A_mem0, A_Wq, A_Wk, A_Wv, A_Wo, A_seg_len)
    g = _silu(_B_ref(xn, B_w1, B_b1, B_w2, B_b2))
    return x + s * g
```

```cpp
#include <hip/hip_runtime.h>
#include <hip/hip_bf16.h>
#include <stdint.h>
#pragma clang diagnostic ignored "-Winline-asm"

typedef unsigned u32;
typedef u32 u32x2 __attribute__((ext_vector_type(2)));
typedef u32 u32x4 __attribute__((ext_vector_type(4)));
typedef u32 u32x8 __attribute__((ext_vector_type(8)));
typedef float f32x4 __attribute__((ext_vector_type(4)));
typedef __bf16 bf16x2 __attribute__((ext_vector_type(2)));
typedef __bf16 bf16x8 __attribute__((ext_vector_type(8)));
typedef __bf16 bf16x16 __attribute__((ext_vector_type(16)));
typedef short s16x4 __attribute__((ext_vector_type(4)));
#define LDSP __attribute__((address_space(3)))
typedef LDSP char* lptr;
#define DEV __device__ __forceinline__

constexpr int SEQ = 2048, DM = 640, NH = 32, DH = 128, NSEG = 64, FF = 1728;
constexpr u32 SENT = 0xFFFFFFFFu;
constexpr u32 WQ_BYTES = 640u * 4096u * 2u, W1_BYTES = 640u * 1728u * 2u;
constexpr int SPIN_CAP = 400000;

constexpr u32 MB = 1u << 20;
constexpr u32 OFF_FLAGS = 0;
constexpr u32 FLAG_BYTES = 128 * 1024;
constexpr u32 OFF_MX = 2 * MB;
constexpr u32 OFF_P = 6 * MB;
constexpr u32 OFF_U = 8 * MB;
constexpr u32 SENT_END = OFF_U + 64u * 32u * 10240u;
constexpr u32 OFF_XN = 32 * MB;
constexpr u32 OFF_G = 36 * MB;
constexpr u32 OFF_H = 40 * MB;
constexpr u32 OFF_PX = 48 * MB;
constexpr u32 OFF_PXM = 54 * MB;
constexpr u32 OFF_Q = 56 * MB;
constexpr u32 OFF_K = 76 * MB;
constexpr u32 OFF_B = 96 * MB;
constexpr u32 WS_USED = 176 * MB;
constexpr int F_QX = 0, F_QG = 8, F_VB = 16  , F_H = 48  , F_G = 64  , F_X = 96  , F_KQ = 256  ;

#define TS(slot, k) do {} while (0)
#define TSV(slot, k, v) do {} while (0)
#define WAIT_V(n) asm volatile("s_waitcnt vmcnt(%0)" ::"n"(n) : "memory")
#define WAIT_L0() asm volatile("s_waitcnt lgkmcnt(0)" ::: "memory")
#define RAW_BARRIER() do { WAIT_L0(); __builtin_amdgcn_s_barrier(); asm volatile("" ::: "memory"); } while (0)

DEV u32 pack2(float a, float b) { bf16x2 v = {(__bf16)a, (__bf16)b}; return __builtin_bit_cast(u32, v); }
DEV float bfr(float a) { return (float)(__bf16)a; }
DEV float blo(u32 d) { return __builtin_bit_cast(float, d << 16); }
DEV float bhi(u32 d) { return __builtin_bit_cast(float, d & 0xffff0000u); }
DEV u32 fbits(float f) { return __builtin_bit_cast(u32, f); }
DEV float bitsf(u32 u) { return __builtin_bit_cast(float, u); }
DEV u32 nosent(u32 d) { return d < 0xFFFFFFFEu ? d : 0xFFFFFFFEu; }
DEV void swap32(u32& a, u32& b) { auto r = __builtin_amdgcn_permlane32_swap(a, b, false, false); a = r[0]; b = r[1]; }
DEV void swap16(u32& a, u32& b) { auto r = __builtin_amdgcn_permlane16_swap(a, b, false, false); a = r[0]; b = r[1]; }
DEV float fold32(float a, float b) { u32 x = fbits(a), y = fbits(b); swap32(x, y); return bitsf(x) + bitsf(y); }
DEV float xmax(float v) {
    u32 a = fbits(v), b = a; swap32(a, b); v = fmaxf(bitsf(a), bitsf(b));
    a = fbits(v); b = a; swap16(a, b); return fmaxf(bitsf(a), bitsf(b));
}
DEV float xsum(float v) {
    u32 a = fbits(v), b = a; swap32(a, b); v = bitsf(a) + bitsf(b);
    a = fbits(v); b = a; swap16(a, b); return bitsf(a) + bitsf(b);
}
DEV u32x2 trread(lptr p) { s16x4 v = __builtin_amdgcn_ds_read_tr16_b64_v4i16((LDSP s16x4*)p); return __builtin_bit_cast(u32x2, v); }
DEV u32x4 lds128(lptr p) { return *(LDSP u32x4*)p; }
DEV u32x4 cat(u32x2 a, u32x2 b) { return (u32x4){a.x, a.y, b.x, b.y}; }
DEV u32x8 cat(u32x4 a, u32x4 b) { return (u32x8){a.x, a.y, a.z, a.w, b.x, b.y, b.z, b.w}; }
DEV f32x4 mfma(u32x4 a, u32x4 b, f32x4 c) {
    return __builtin_amdgcn_mfma_f32_16x16x32_bf16(__builtin_bit_cast(bf16x8, a), __builtin_bit_cast(bf16x8, b), c, 0, 0, 0);
}
DEV f32x4 smfmac(u32x4 a, u32x8 b, f32x4 c, int idx) {
    return __builtin_amdgcn_smfmac_f32_16x16x64_bf16(__builtin_bit_cast(bf16x8, a), __builtin_bit_cast(bf16x16, b), c, idx, 0, 0);
}
template <int SC1> DEV void bdma(u32x4 rs, u32 vo, u32 so, u32 lo) {
    if (SC1) asm volatile("s_mov_b32 m0, %3\n\ts_nop 3\n\tbuffer_load_dwordx4 %0, %1, %2 offen sc1 lds" ::"v"(vo), "s"(rs), "s"(so), "s"(lo) : "memory", "m0");
    else asm volatile("s_mov_b32 m0, %3\n\ts_nop 3\n\tbuffer_load_dwordx4 %0, %1, %2 offen lds" ::"v"(vo), "s"(rs), "s"(so), "s"(lo) : "memory", "m0");
}
DEV u32x4 mkrsrc(const void* p, u32 bytes) {
    unsigned long long a = (unsigned long long)p;
    u32x4 r = {(u32)a, (u32)(a >> 32) & 0xffffu, bytes, 0x00020000u};
    r.x = __builtin_amdgcn_readfirstlane(r.x); r.y = __builtin_amdgcn_readfirstlane(r.y);
    r.z = __builtin_amdgcn_readfirstlane(r.z); r.w = __builtin_amdgcn_readfirstlane(r.w);
    return r;
}
DEV u32 max4(u32x4 v) { u32 a = v.x > v.y ? v.x : v.y, b = v.z > v.w ? v.z : v.w; return a > b ? a : b; }
DEV u32 umax(u32 a, u32 b) { return a > b ? a : b; }

typedef __amdgpu_buffer_rsrc_t rsrc_t;
template <int AUX> DEV void bst128(rsrc_t r, u32x4 v, u32 off) { __builtin_amdgcn_raw_buffer_store_b128(v, r, off, 0, AUX); }
template <int AUX> DEV void bst64(rsrc_t r, u32x2 v, u32 off) { __builtin_amdgcn_raw_buffer_store_b64(v, r, off, 0, AUX); }
template <int AUX> DEV void bst32(rsrc_t r, u32 v, u32 off) { __builtin_amdgcn_raw_buffer_store_b32(v, r, off, 0, AUX); }
DEV u32x4 bld128s(rsrc_t r, u32 off) { return __builtin_amdgcn_raw_buffer_load_b128(r, off, 0, 16); }

DEV void wait_flag(u32* flags, int idx, u32 target, bool& dead) {
    if (dead) return;
    int spins = 0;
    for (;;) {
        u32 v = __hip_atomic_load(flags + idx * 32, __ATOMIC_RELAXED, __HIP_MEMORY_SCOPE_AGENT);
        if (__builtin_amdgcn_readfirstlane(v) >= target) break;
        if (++spins > SPIN_CAP) { dead = true; break; }
        __builtin_amdgcn_s_sleep(2);
    }
    asm volatile("" ::: "memory");
}
DEV void flag_add(u32* flags, int idx) { __hip_atomic_fetch_add(flags + idx * 32, 1u, __ATOMIC_RELAXED, __HIP_MEMORY_SCOPE_AGENT); }

__global__ void __launch_bounds__(256) prologue_kernel(const u32* __restrict__ x, char* __restrict__ ws) {
    const int tid = threadIdx.x, lane = tid & 63, wid = tid >> 6;
    const int row = blockIdx.x * 4 + wid;
    u32 d[5];
#pragma unroll
    for (int i = 0; i < 5; ++i) d[i] = x[row * 320 + i * 64 + lane];
    {
        const u32x4 s = {SENT, SENT, SENT, SENT};
        constexpr u32 n16 = (SENT_END - OFF_MX) / 16;
        u32x4* p = (u32x4*)(ws + OFF_MX);
        for (u32 i = blockIdx.x * 256 + tid; i < n16; i += 512 * 256) p[i] = s;
        const u32x4 z = {0, 0, 0, 0};
        u32 j = blockIdx.x * 256 + tid;
        if (j < FLAG_BYTES / 16) ((u32x4*)(ws + OFF_FLAGS))[j] = z;
    }
    float v[10], s = 0.f;
#pragma unroll
    for (int i = 0; i < 5; ++i) { v[2 * i] = blo(d[i]); v[2 * i + 1] = bhi(d[i]); s += v[2 * i] + v[2 * i + 1]; }
#pragma unroll
    for (int o = 32; o; o >>= 1) s += __shfl_xor(s, o);
    const float mean = s * (1.0f / DM);
    float q = 0.f;
#pragma unroll
    for (int i = 0; i < 10; ++i) { float t = v[i] - mean; q += t * t; }
#pragma unroll
    for (int o = 32; o; o >>= 1) q += __shfl_xor(q, o);
    const float mb = bfr(mean), vb = bfr(q * (1.0f / DM));
    const float den = bfr(sqrtf(bfr(vb + 1e-5f)));
    u32* xn = (u32*)(ws + OFF_XN);
#pragma unroll
    for (int i = 0; i < 5; ++i) xn[row * 320 + i * 64 + lane] = pack2(bfr(v[2 * i] - mb) / den, bfr(v[2 * i + 1] - mb) / den);
}

template <int NT>
DEV void gemm_core(f32x4 (&acc)[4][NT], u32x4 rX, u32 xoff, int ldx, u32x4 rW0, u32x4 rW1, u32 woff0, u32 woff1, int ldw, int chmax1, int nkt,
                   lptr lds, int wid, int lane) {
    constexpr int NP = NT / 2, STAGE = 16384 * (1 + NP), G = 2 + 2 * NP;
    const int sb = lane * 16, swz = sb ^ (((sb >> 9) & 1) << 5);
    const u32 xv = (u32)(16 * wid + (swz >> 6)) * ldx + (swz & 63);
    u32 wv0[2], wv1[2];
#pragma unroll
    for (int i = 0; i < 2; ++i) {
        const int row = 8 * wid + 4 * i + (lane >> 4);
        const int ch = (lane & 15) ^ (((row & 3) << 2) | ((row >> 2) & 3));
        wv0[i] = (u32)row * ldw + ch * 16;
        wv1[i] = (u32)row * ldw + (ch < chmax1 ? ch : chmax1) * 16;
    }
    const u32 ldsb = (u32)(size_t)lds;
    auto issue = [&](int kt, int buf) {
        const u32 b = ldsb + buf * STAGE;
        bdma<1>(rX, xv, xoff + kt * 128, b + wid * 2048);
        bdma<1>(rX, xv, xoff + kt * 128 + 64, b + wid * 2048 + 1024);
        const u32 wk = (u32)kt * 64u * ldw;
#pragma unroll
        for (int i = 0; i < 2; ++i) {
            bdma<0>(rW0, wv0[i], woff0 + wk, b + 16384 + (8 * wid + 4 * i) * 256);
            if (NP == 2) bdma<0>(rW1, wv1[i], woff1 + wk, b + 32768 + (8 * wid + 4 * i) * 256);
        }
    };
    const int wr = wid >> 2, wc = wid & 3, g = lane >> 4, q = (lane >> 2) & 3, p = lane & 3;
    const int ob = (lane & 15) * 64 + g * 16;
    const int xo = wr * 8192 + (ob ^ (((ob >> 9) & 1) << 5));
    int wo[NT][2];
#pragma unroll
    for (int n = 0; n < NT; ++n)
#pragma unroll
        for (int t = 0; t < 2; ++t) {
            const int panel = (NT == 4) ? (wc >> 1) : 0;
            const int c = (NT == 4) ? ((wc & 1) * 4 + n) : (wc * 2 + n);
            wo[n][t] = 16384 * (1 + panel) + 256 * (8 * g + 4 * t + q) + 16 * ((2 * c + (p >> 1)) ^ ((q << 2) | ((2 * g + t) & 3))) + 8 * (p & 1);
        }
#pragma unroll
    for (int m = 0; m < 4; ++m)
#pragma unroll
        for (int n = 0; n < NT; ++n) acc[m][n] = (f32x4){0.f, 0.f, 0.f, 0.f};
    issue(0, 0);
    issue(1, 1);
    int buf = 0;
    for (int kt = 0; kt < nkt; ++kt) {
        if (kt + 1 < nkt) WAIT_V(G); else WAIT_V(0);
        RAW_BARRIER();
        if (kt + 2 < nkt) issue(kt + 2, buf >= 1 ? buf - 1 : 2);
        lptr b = lds + buf * STAGE;
#pragma unroll
        for (int ks = 0; ks < 2; ++ks) {
            u32x4 xf[4], wf[NT];
#pragma unroll
            for (int m = 0; m < 4; ++m) xf[m] = lds128(b + xo + (m * 2 + ks) * 1024);
#pragma unroll
            for (int n = 0; n < NT; ++n) wf[n] = cat(trread(b + wo[n][0] + ks * 8192), trread(b + wo[n][1] + ks * 8192));
#pragma unroll
            for (int m = 0; m < 4; ++m)
#pragma unroll
                for (int n = 0; n < NT; ++n) acc[m][n] = mfma(wf[n], xf[m], acc[m][n]);
        }
        buf = (buf == 2) ? 0 : buf + 1;
    }
}
DEV u32x4 pair8(u32 a0, u32 a1, u32 b0, u32 b1) { swap16(a0, b0); swap16(a1, b1); return (u32x4){a0, a1, b0, b1}; }

struct Args {
    const char *x, *mem0, *Wq, *Wk, *Wv, *Wo, *w1, *b1, *w2, *b2;
    char *out, *ws;
};

DEV void worker(const Args& a, lptr lds, LDSP int* bc, int wid, int lane, int tid) {
    char* ws = a.ws;
    u32* flags = (u32*)(ws + OFF_FLAGS);
    const rsrc_t rws = __builtin_amdgcn_make_buffer_rsrc(ws, 0, WS_USED, 0x00020000);
    const u32x4 rs = mkrsrc(ws, WS_USED);
    const u32 ldsb = (u32)(size_t)lds;
    const int xq = (blockIdx.x & 7) - 2;
    const int wr = wid >> 2, wc = wid & 3, g = lane >> 4, fr = lane & 15;
    const int c8 = 8 * ((g >> 1) + 2 * (g & 1));
    int qi = 0;
    bool dead = false;
    int nit = 0;
    for (;;) {
        if (wid == 0 && nit < 80) TS(nit, 0);
        if (tid == 0) {
            int type = -1, p0 = 0, p1 = 0;
            while (qi < 6) {
                int qq = xq + qi; if (qq >= 6) qq -= 6;
                const int nu = qq < 4 ? 11 : 10;
                const int it = (int)__hip_atomic_fetch_add(flags + (F_QX + qq) * 32, 1u, __ATOMIC_RELAXED, __HIP_MEMORY_SCOPE_AGENT);
                if (it < nu * 16) { const int u = qq + 6 * (it % nu); type = u & 1; p0 = it / nu; p1 = u >> 1; break; }
                ++qi;
            }
            if (type < 0) {
                const int it = (int)__hip_atomic_fetch_add(flags + F_QG * 32, 1u, __ATOMIC_RELAXED, __HIP_MEMORY_SCOPE_AGENT);
                if (it < 112) { type = 2; p0 = it / 7; p1 = it % 7; }
                else if (it < 192) { type = 3; p0 = (it - 112) / 5; p1 = (it - 112) % 5; }
                else if (it < 192 + 640) { type = 4; p0 = (it - 192) / 10; p1 = (it - 192) % 10; }
            }
            bc[0] = type; bc[1] = p0; bc[2] = p1;
        }
        RAW_BARRIER();
        const int type = __builtin_amdgcn_readfirstlane(bc[0]), p0 = __builtin_amdgcn_readfirstlane(bc[1]), p1 = __builtin_amdgcn_readfirstlane(bc[2]);
        RAW_BARRIER();
        if (type < 0) break;
        if (wid == 0 && nit < 80) { TS(nit, 1); TSV(nit, 2, type * 65536 + p0 * 256 + p1 + 1); }
        ++nit;
        if (type == 0 || type == 2) {
            f32x4 acc[4][4];
            {
                const bool qk = type == 0;
                const u32x4 r0 = mkrsrc(qk ? a.Wq : a.w1, qk ? WQ_BYTES : W1_BYTES), r1 = mkrsrc(qk ? a.Wk : a.w1, qk ? WQ_BYTES : W1_BYTES);
                const u32 o0 = qk ? p1 * 256 : p1 * 512, o1 = qk ? o0 : o0 + 256;
                gemm_core<4>(acc, rs, OFF_XN + (u32)p0 * 128u * 1280u, 1280, r0, r1, o0, o1, qk ? 8192 : FF * 2, (!qk && p1 == 6) ? 7 : 15, 10, lds, wid, lane);
            }
            if (type == 0) {
                const u32 base = ((wc >> 1) ? OFF_K : OFF_Q) + (u32)(p0 * 128 + wr * 64 + fr) * 8192u + p1 * 256 + ((wc & 1) * 64 + c8) * 2;
#pragma unroll
                for (int m = 0; m < 4; ++m)
#pragma unroll
                    for (int n = 0; n < 4; n += 2) {
                        u32x4 v = pair8(pack2(acc[m][n][0], acc[m][n][1]), pack2(acc[m][n][2], acc[m][n][3]),
                                        pack2(acc[m][n + 1][0], acc[m][n + 1][1]), pack2(acc[m][n + 1][2], acc[m][n + 1][3]));
                        bst128<16>(rws, v, base + m * 16 * 8192 + n * 32);
                    }
            } else {
                const int col0 = p1 * 256 + wc * 64;
                u32x2 bb[4];
#pragma unroll
                for (int n = 0; n < 4; ++n) { int c = col0 + n * 16 + 4 * g; c = c < FF - 4 ? c : FF - 4; bb[n] = *(const u32x2*)(a.b1 + c * 2); }
                const u32 base = OFF_H + (u32)(p0 * 128 + wr * 64 + fr) * (FF * 2) + (col0 + c8) * 2;
#pragma unroll
                for (int m = 0; m < 4; ++m) {
                    u32 h[4][2];
#pragma unroll
                    for (int n = 0; n < 4; ++n) {
                        float t0 = fmaxf(bfr(bfr(acc[m][n][0]) + blo(bb[n].x)), 0.f), t1 = fmaxf(bfr(bfr(acc[m][n][1]) + bhi(bb[n].x)), 0.f);
                        float t2 = fmaxf(bfr(bfr(acc[m][n][2]) + blo(bb[n].y)), 0.f), t3 = fmaxf(bfr(bfr(acc[m][n][3]) + bhi(bb[n].y)), 0.f);
                        h[n][0] = pack2(t0 * t0, t1 * t1); h[n][1] = pack2(t2 * t2, t3 * t3);
                    }
#pragma unroll
                    for (int n = 0; n < 4; n += 2) {
                        u32x4 v = pair8(h[n][0], h[n][1], h[n + 1][0], h[n + 1][1]);
                        if (col0 < FF) bst128<16>(rws, v, base + m * 16 * (FF * 2) + n * 32);
                    }
                }
            }
            WAIT_V(0);
            RAW_BARRIER();
            if (tid == 0) flag_add(flags, type == 0 ? F_KQ + p0 * 32 + p1 : F_H + p0);
        } else if (type == 1 || type == 3) {
            f32x4 acc[4][2];
            u32 wop[4];
            const u32x4 rWo = mkrsrc(a.Wo, WQ_BYTES);
            const u32 wob = (u32)p1 * 128u * 1280u;
            if (type == 1) {
#pragma unroll
                for (int i = 0; i < 4; ++i) {
                    const int row = 16 * wid + 4 * i + g;
                    const int ch = fr ^ (((row & 3) << 2) | ((row >> 2) & 3));
                    wop[i] = (u32)row * 1280u + ch * 16;
                    bdma<0>(rWo, wop[i], wob, ldsb + 3 * 32768 + (16 * wid + 4 * i) * 256);
                }
            } else {
                wait_flag(flags, F_H + p0, 7, dead);
            }
            {
                const bool v = type == 1;
                const u32x4 r0 = mkrsrc(v ? a.Wv : a.w2, v ? WQ_BYTES : W1_BYTES);
                gemm_core<2>(acc, rs, v ? OFF_XN + (u32)p0 * 128u * 1280u : OFF_H + (u32)p0 * 128u * (FF * 2), v ? 1280 : FF * 2, r0, r0, p1 * 256, 0,
                             v ? 8192 : 1280, 15, v ? 10 : 27, lds, wid, lane);
            }
            if (type == 1) {
                {
                    const int ob = fr * 64 + c8 * 2;
                    lptr vi = lds + 4 * 32768 + wc * 1024 + (ob ^ (((ob >> 9) & 1) << 5));
#pragma unroll
                    for (int m = 0; m < 4; ++m)
                        *(LDSP u32x4*)(vi + (wr * 4 + m) * 4096) = pair8(pack2(acc[m][0][0], acc[m][0][1]), pack2(acc[m][0][2], acc[m][0][3]),
                                                                          pack2(acc[m][1][0], acc[m][1][1]), pack2(acc[m][1][2], acc[m][1][3]));
                }
                RAW_BARRIER();
#pragma unroll
                for (int pn = 1; pn < 4; ++pn)
#pragma unroll
                    for (int i = 0; i < 4; ++i) bdma<0>(rWo, wop[i], wob + pn * 256, ldsb + (pn - 1) * 32768 + (16 * wid + 4 * i) * 256);
                u32x4 vf[4][4];
                {
                    const int ob = fr * 64 + g * 16;
                    lptr vi = lds + 4 * 32768 + wr * 16384 + (ob ^ (((ob >> 9) & 1) << 5));
#pragma unroll
                    for (int m = 0; m < 4; ++m)
#pragma unroll
                        for (int ks = 0; ks < 4; ++ks) vf[m][ks] = lds128(vi + m * 4096 + ks * 1024);
                }
                RAW_BARRIER();
#pragma unroll
                for (int i = 0; i < 4; ++i) bdma<0>(rWo, wop[i], wob + 4 * 256, ldsb + 4 * 32768 + (16 * wid + 4 * i) * 256);
                const int q = (lane >> 2) & 3, p = lane & 3;
                int wo[2][2];
#pragma unroll
                for (int n = 0; n < 2; ++n)
#pragma unroll
                    for (int t = 0; t < 2; ++t)
                        wo[n][t] = 256 * (8 * g + 4 * t + q) + 16 * ((2 * (wc * 2 + n) + (p >> 1)) ^ ((q << 2) | ((2 * g + t) & 3))) + 8 * (p & 1);
                for (int pn = 0; pn < 5; ++pn) {
                    if (pn) { WAIT_V(16); RAW_BARRIER(); }
                    lptr b = lds + (pn == 0 ? 3 : (pn == 4 ? 4 : pn - 1)) * 32768;
                    f32x4 c2[4][2];
#pragma unroll
                    for (int m = 0; m < 4; ++m) { c2[m][0] = (f32x4){0.f, 0.f, 0.f, 0.f}; c2[m][1] = c2[m][0]; }
#pragma unroll
                    for (int ks = 0; ks < 4; ++ks) {
                        u32x4 wf[2];
#pragma unroll
                        for (int n = 0; n < 2; ++n) wf[n] = cat(trread(b + wo[n][0] + ks * 8192), trread(b + wo[n][1] + ks * 8192));
#pragma unroll
                        for (int m = 0; m < 4; ++m)
#pragma unroll
                            for (int n = 0; n < 2; ++n) c2[m][n] = mfma(vf[m][ks], wf[n], c2[m][n]);
                    }
#pragma unroll
                    for (int sg = 0; sg < 2; ++sg)
#pragma unroll
                        for (int n = 0; n < 2; ++n) {
                            u32x4 v = {pack2(c2[2 * sg][n][0], c2[2 * sg][n][1]), pack2(c2[2 * sg][n][2], c2[2 * sg][n][3]),
                                       pack2(c2[2 * sg + 1][n][0], c2[2 * sg + 1][n][1]), pack2(c2[2 * sg + 1][n][2], c2[2 * sg + 1][n][3])};
                            const u32 seg = p0 * 4 + wr * 2 + sg;
                            bst128<16>(rws, v, OFF_B + ((seg * 32 + p1) * 40 + pn * 8 + wc * 2 + n) * 1024u + lane * 16);
                        }
                }
            } else {
                const int col0 = p1 * 128 + wc * 32;
                u32x2 bb[2];
#pragma unroll
                for (int n = 0; n < 2; ++n) bb[n] = *(const u32x2*)(a.b2 + (col0 + n * 16 + 4 * g) * 2);
                const u32 base = OFF_G + (u32)(p0 * 128 + wr * 64 + fr) * 1280u + (col0 + c8) * 2;
#pragma unroll
                for (int m = 0; m < 4; ++m) {
                    u32 h[2][2];
#pragma unroll
                    for (int n = 0; n < 2; ++n) {
                        float t[4] = {bfr(bfr(acc[m][n][0]) + blo(bb[n].x)), bfr(bfr(acc[m][n][1]) + bhi(bb[n].x)),
                                      bfr(bfr(acc[m][n][2]) + blo(bb[n].y)), bfr(bfr(acc[m][n][3]) + bhi(bb[n].y))};
#pragma unroll
                        for (int r = 0; r < 4; ++r) t[r] = t[r] / (1.0f + __expf(-t[r]));
                        h[n][0] = pack2(t[0], t[1]); h[n][1] = pack2(t[2], t[3]);
                    }
                    bst128<16>(rws, pair8(h[0][0], h[0][1], h[1][0], h[1][1]), base + m * 16 * 1280);
                }
            }
            WAIT_V(0);
            RAW_BARRIER();
            if (tid == 0) flag_add(flags, type == 1 ? F_VB + p0 * 2 + (p1 >> 4) : F_G + p0);
        } else {
            const int seg = p0, cg = p1;
            wait_flag(flags, F_VB + (seg >> 2) * 2 + (wid >> 2), 16, dead);
            u32x4 yb[4][4], yu[4], px[4][2], pm[2];
#pragma unroll
            for (int hh = 0; hh < 4; ++hh)
#pragma unroll
                for (int n = 0; n < 4; ++n) yb[hh][n] = bld128s(rws, OFF_B + ((seg * 32 + wid * 4 + hh) * 40 + cg * 4 + n) * 1024u + lane * 16);
            wait_flag(flags, F_G + (seg >> 2), 5, dead);
            const u32 eo = (u32)(seg * 32 + (wid >> 2) * 16 + fr) * 1280u + (cg * 64 + (wid & 3) * 16 + 4 * g) * 2;
            const u32x2 xv = *(const u32x2*)(a.x + eo);
            const u32x2 gv = __builtin_amdgcn_raw_buffer_load_b64(rws, OFF_G + eo, 0, 16);
            wait_flag(flags, F_X + seg, 96, dead);
#pragma unroll
            for (int hh = 0; hh < 4; ++hh)
#pragma unroll
                for (int qt = 0; qt < 2; ++qt) px[hh][qt] = bld128s(rws, OFF_PX + (seg * 32 + wid * 4 + hh) * 2048u + qt * 1024 + lane * 16);
#pragma unroll
            for (int qt = 0; qt < 2; ++qt) pm[qt] = bld128s(rws, OFF_PXM + (seg * 8 + wid) * 2048u + qt * 1024 + lane * 16);
#pragma unroll
            for (int n = 0; n < 4; ++n) yu[n] = bld128s(rws, OFF_U + (seg * 32 + wid * 4 + g) * 10240u + (cg * 64 + n * 16 + fr) * 16);
            f32x4 c[2][4];
#pragma unroll
            for (int qt = 0; qt < 2; ++qt)
#pragma unroll
                for (int n = 0; n < 4; ++n) {
                    f32x4 t = {0.f, 0.f, 0.f, 0.f};
#pragma unroll
                    for (int hh = 0; hh < 4; ++hh) t = mfma(yb[hh][n], px[hh][qt], t);
                    c[qt][n] = mfma(yu[n], pm[qt], t);
                }
#pragma unroll
            for (int qt = 0; qt < 2; ++qt)
#pragma unroll
                for (int n = 0; n < 4; ++n) *(LDSP f32x4*)(lds + (wid * 8 + qt * 4 + n) * 1024 + lane * 16) = c[qt][n];
            RAW_BARRIER();
            f32x4 s = {0.f, 0.f, 0.f, 0.f};
#pragma unroll
            for (int w = 0; w < 8; ++w) s += *(LDSP f32x4*)(lds + (w * 8 + wid) * 1024 + lane * 16);
            u32x2 o;
            o.x = pack2(blo(xv.x) + bfr(bfr(s[0]) * blo(gv.x)), bhi(xv.x) + bfr(bfr(s[1]) * bhi(gv.x)));
            o.y = pack2(blo(xv.y) + bfr(bfr(s[2]) * blo(gv.y)), bhi(xv.y) + bfr(bfr(s[3]) * bhi(gv.y)));
            *(u32x2*)(a.out + eo) = o;
        }
    }
}

#define BL4 "buffer_load_dwordx4 "
DEV void ld4(u32x4 rs, u32 vo, u32 s0, u32 s1, u32x4 (&t)[4]) {
    asm volatile("s_nop 4\n\t" BL4 "%0, %4, %5, %6 offen sc1\n\t" BL4 "%1, %4, %5, %6 offen offset:1024 sc1\n\t"
                 BL4 "%2, %4, %5, %7 offen sc1\n\t" BL4 "%3, %4, %5, %7 offen offset:1024 sc1\n\ts_waitcnt vmcnt(0)"
                 : "=&v"(t[0]), "=&v"(t[1]), "=&v"(t[2]), "=&v"(t[3]) : "v"(vo), "s"(rs), "s"(s0), "s"(s1) : "memory");
}
DEV void ld4v(u32x4 rs, u32 v0, u32 v1, u32 v2, u32 v3, u32 s0, u32x4 (&t)[4]) {
    asm volatile("s_nop 4\n\t" BL4 "%0, %4, %8, %9 offen sc1\n\t" BL4 "%1, %5, %8, %9 offen sc1\n\t"
                 BL4 "%2, %6, %8, %9 offen sc1\n\t" BL4 "%3, %7, %8, %9 offen sc1\n\ts_waitcnt vmcnt(0)"
                 : "=&v"(t[0]), "=&v"(t[1]), "=&v"(t[2]), "=&v"(t[3]) : "v"(v0), "v"(v1), "v"(v2), "v"(v3), "s"(rs), "s"(s0) : "memory");
}
DEV void ld3v(u32x4 rs, u32 v0, u32 v2, u32 s0, u32x4 (&t)[3]) {
    asm volatile("s_nop 4\n\t" BL4 "%0, %3, %5, %6 offen sc1\n\t" BL4 "%1, %3, %5, %6 offen offset:1024 sc1\n\t"
                 BL4 "%2, %4, %5, %6 offen sc1\n\ts_waitcnt vmcnt(0)"
                 : "=&v"(t[0]), "=&v"(t[1]), "=&v"(t[2]) : "v"(v0), "v"(v2), "s"(rs), "s"(s0) : "memory");
}
DEV u32 add2(u32 a, u32 b) { return pack2(blo(a) + blo(b), bhi(a) + bhi(b)); }
DEV u32x4 add8(u32x4 a, u32x4 b) { return (u32x4){add2(a.x, b.x), add2(a.y, b.y), add2(a.z, b.z), add2(a.w, b.w)}; }
DEV int memi_unit(int k) { return k ^ (((k >> 4) & 1) << 3); }

constexpr int NPRE = 8;
constexpr int L_MEMI = 0, L_QKI = 10240, L_PI = 16384, L_RING = 32768;

DEV void gather_mem(const Args& a, u32x4 rs, lptr lds, int s, int wid, int lane, int tid, bool& dead) {
    if (s == 0) {
        for (int k = tid; k < DM; k += 512) {
            const unsigned short* m = (const unsigned short*)a.mem0 + k;
            u32x4 v;
            v.x = m[0] | ((u32)m[DM] << 16); v.y = m[2 * DM] | ((u32)m[3 * DM] << 16);
            v.z = m[4 * DM] | ((u32)m[5 * DM] << 16); v.w = m[6 * DM] | ((u32)m[7 * DM] << 16);
            *(LDSP u32x4*)(lds + L_MEMI + memi_unit(k) * 16) = v;
        }
    } else if (wid < 5) {
        u32x4 t[4];
        const u32 s0 = OFF_MX + (u32)s * 20480u;
        int spins = 0;
        for (;;) {
            ld4(rs, wid * 2048 + lane * 16, s0, s0 + 10240, t);
            const u32 mx = umax(umax(max4(t[0]), max4(t[1])), umax(max4(t[2]), max4(t[3])));
            if (__all(mx != SENT) || dead) break;
            if (++spins > SPIN_CAP) dead = true;
        }
        const int u = memi_unit(wid * 128 + lane);
        *(LDSP u32x4*)(lds + L_MEMI + u * 16) = add8(t[0], t[2]);
        *(LDSP u32x4*)(lds + L_MEMI + u * 16 + 1024) = add8(t[1], t[3]);
    }
}
DEV int a_off(int lane) {
    const int G = lane >> 4, q = (lane >> 2) & 3, p = lane & 3;
    return 16 * memi_unit(16 * G + 4 * (q >> 1) + (q & 1) + 2 * (p >> 1)) + 8 * (p & 1);
}
DEV void stage_rows(u32x4 r, u32 off, int ld, int rowb, int rows, lptr dst, int wid, int lane) {
    const int total = rows * rowb / 1024;
    for (int i = wid; i < total; i += 8) {
        const int b = i * 1024 + lane * 16;
        bdma<0>(r, (u32)(b / rowb) * ld + (b % rowb), off, (u32)(size_t)dst + i * 1024);
    }
}
DEV u32x8 wfrag(lptr img, int rowb, int k0, int col0, int lane) {
    const int g = lane >> 4, q = (lane >> 2) & 3, p = lane & 3;
    lptr b = img + (k0 + 8 * g + q) * rowb + (col0 + 4 * p) * 2;
    return cat(cat(trread(b), trread(b + 4 * rowb)), cat(trread(b + 32 * rowb), trread(b + 36 * rowb)));
}

DEV void chain_s(const Args& a, lptr lds, int h, int wid, int lane, int tid) {
    char* ws = a.ws;
    u32* flags = (u32*)(ws + OFF_FLAGS);
    const rsrc_t rws = __builtin_amdgcn_make_buffer_rsrc(ws, 0, WS_USED, 0x00020000);
    const u32x4 rs = mkrsrc(ws, WS_USED);
    const int g = lane >> 4, fr = lane & 15;
    bool dead = false;
    u32x8 W[2][10];
    {
        const u32x4 rq = mkrsrc(a.Wq, WQ_BYTES), rk = mkrsrc(a.Wk, WQ_BYTES);
        auto st = [&](int pc) {
            lptr d = lds + (pc & 1) * 65536;
            stage_rows(rq, (u32)pc * 128u * 8192u + h * 256, 8192, 256, 128, d, wid, lane);
            stage_rows(rk, (u32)pc * 128u * 8192u + h * 256, 8192, 256, 128, d + 32768, wid, lane);
        };
        st(0);
#pragma unroll
        for (int pc = 0; pc < 5; ++pc) {
            if (pc + 1 < 5) { st(pc + 1); WAIT_V(8); } else WAIT_V(0);
            RAW_BARRIER();
            lptr img = lds + (pc & 1) * 65536 + (wid >> 2) * 32768;
#pragma unroll
            for (int c = 0; c < 2; ++c)
#pragma unroll
                for (int t = 0; t < 2; ++t) W[t][pc * 2 + c] = wfrag(img, 256, c * 64, (wid & 3) * 32 + t * 16, lane);
            RAW_BARRIER();
        }
    }
    const int key0 = 16 * ((fr & 3) >> 1) + 8 * (fr >> 3) + 2 * ((fr >> 2) & 1) + (fr & 1);
    const u32 ldsb = (u32)(size_t)lds;
    auto dma = [&](int seg) {
        const u32 d = ldsb + L_RING + (seg & 1) * 16384;
        const u32 kv = key0 * 8192 + g * 16, qv = fr * 8192 + g * 16;
        const u32 sb = (u32)seg * 32u * 8192u + h * 256;
#pragma unroll
        for (int t = 0; t < 2; ++t)
#pragma unroll
            for (int c = 0; c < 4; ++c) {
                bdma<1>(rs, kv, OFF_K + sb + t * 4 * 8192 + c * 64, d + (t * 4 + c) * 1024);
                bdma<1>(rs, qv, OFF_Q + sb + t * 16 * 8192 + c * 64, d + 8192 + (t * 4 + c) * 1024);
            }
    };
    if (wid == 7) { wait_flag(flags, F_KQ + h, 1, dead); dma(0); dma(1); }
    const int idx = (fr < 8) ? 0x4444 : 0xEEEE;
    const int ao = a_off(lane);
    const float cs = 0.08838834764831845f * 1.4426950408889634f;
    const int grp = h >> 4, hl = h & 15;
    for (int s = 0; s < NSEG; ++s) {
        if (wid == 0) TS(s, 0);
        gather_mem(a, rs, lds, s, wid, lane, tid, dead);
        if (wid == 0) TS(s, 1);
        if (wid == 7) WAIT_V(0);
        RAW_BARRIER();
        if (wid == 0) TS(s, 2);
        f32x4 c0 = {0.f, 0.f, 0.f, 0.f}, c1 = c0;
#pragma unroll
        for (int c = 0; c < 10; ++c) {
            const u32x4 av = cat(trread(lds + L_MEMI + c * 1024 + ao), trread(lds + L_MEMI + c * 1024 + (ao ^ 128)));
            c0 = smfmac(av, W[0][c], c0, idx);
            c1 = smfmac(av, W[1][c], c1, idx);
        }
        {
            const u32 d0 = pack2(fold32(c0[0], c1[0]), fold32(c0[1], c1[1])), d1 = pack2(fold32(c0[2], c1[2]), fold32(c0[3], c1[3]));
            LDSP u32* qi = (LDSP u32*)(lds + L_QKI + (wid * 32 + (lane >> 5) * 16 + fr) * 16 + (g & 1) * 4);
            qi[0] = d0; qi[2] = d1;
        }
        RAW_BARRIER();
        if (wid == 0) TS(s, 3);
        if (wid < 3) {
            lptr rg = lds + L_RING + (s & 1) * 16384;
            lptr qk = lds + L_QKI + (8 * g + ((lane >> 2) & 3)) * 16 + 8 * (lane & 1);
            f32x4 st[3];
#pragma unroll
            for (int t = 0; t < 3; ++t) st[t] = (f32x4){0.f, 0.f, 0.f, 0.f};
#pragma unroll
            for (int c = 0; c < 4; ++c) {
                u32x4 qf;
                if (wid == 0) qf = cat(trread(qk + c * 512), trread(qk + c * 512 + 64));
                else qf = lds128(rg + 8192 + ((wid - 1) * 4 + c) * 1024 + lane * 16);
                const u32x4 km = cat(trread(qk + 2048 + c * 512), trread(qk + 2048 + c * 512 + 64));
                st[0] = mfma(lds128(rg + c * 1024 + lane * 16), qf, st[0]);
                st[1] = mfma(lds128(rg + (4 + c) * 1024 + lane * 16), qf, st[1]);
                st[2] = mfma(km, qf, st[2]);
            }
            const float NEG = -3.0e38f;
            if (wid) {
                const int tq = (wid - 1) * 16 + fr;
#pragma unroll
                for (int t = 0; t < 2; ++t)
#pragma unroll
                    for (int r = 0; r < 4; ++r) {
                        const int key = 16 * (r >> 1) + 8 * (g >> 1) + 4 * t + 2 * (g & 1) + (r & 1);
                        st[t][r] = key <= tq ? st[t][r] : NEG;
                    }
            }
            float m = fmaxf(fmaxf(fmaxf(st[0][0], st[0][1]), fmaxf(st[0][2], st[0][3])), fmaxf(fmaxf(st[1][0], st[1][1]), fmaxf(st[1][2], st[1][3])));
            m = fmaxf(m, fmaxf(fmaxf(st[2][0], st[2][1]), fmaxf(st[2][2], st[2][3])));
            m = xmax(m);
            const float mc = m * cs;
            float sum = 0.f;
#pragma unroll
            for (int t = 0; t < 3; ++t)
#pragma unroll
                for (int r = 0; r < 4; ++r) {
                    const float e = __builtin_amdgcn_exp2f(__builtin_fmaf(st[t][r], cs, -mc));
                    st[t][r] = e;
                    sum += (t == 2 && g >= 2) ? 0.f : e;
                }
            sum = xsum(sum);
            const float inv = 1.0f / sum;
            u32 d[3][2];
#pragma unroll
            for (int t = 0; t < 3; ++t) { d[t][0] = pack2(st[t][0] * inv, st[t][1] * inv); d[t][1] = pack2(st[t][2] * inv, st[t][3] * inv); }
            if (wid == 0) {
                if (s + 1 < NSEG) {
                    const int qi = 4 * ((fr >> 1) & 1) + 2 * ((fr >> 2) & 1) + (fr & 1);
                    const u32 pb = OFF_P + (u32)(s * 2 + grp) * 10240u;
                    if (fr < 8) {
                        bst128<16>(rws, (u32x4){nosent(d[0][0]), nosent(d[0][1]), nosent(d[1][0]), nosent(d[1][1])}, pb + hl * 512 + (8 * g + qi) * 16);
                        if (g < 2) bst64<16>(rws, (u32x2){nosent(d[2][0]), nosent(d[2][1])}, pb + 8192 + hl * 128 + (8 * g + qi) * 8);
                    }
                    TS(s, 4);
                }
            } else {
                swap16(d[0][0], d[1][0]); swap16(d[0][1], d[1][1]);
                bst128<16>(rws, (u32x4){d[0][0], d[1][0], d[0][1], d[1][1]}, OFF_PX + (u32)(s * 32 + h) * 2048u + (wid - 1) * 1024 + lane * 16);
                u32 e0 = d[2][0], e1 = d[2][1];
                swap16(d[2][0], e0); swap16(d[2][1], e1);
                if (g == 0) bst128<16>(rws, (u32x4){d[2][0], e0, d[2][1], e1}, OFF_PXM + (u32)(s * 8 + (h >> 2)) * 2048u + (wid - 1) * 1024 + ((h & 3) * 16 + fr) * 16);
                WAIT_V(0);
                if (lane == 0) flag_add(flags, F_X + s);
            }
        }
        if (wid == 7 && s >= 1 && s + 1 < NSEG) {
            if (((s + 1) & 3) == 0) wait_flag(flags, F_KQ + ((s + 1) >> 2) * 32 + h, 1, dead);
            dma(s + 1);
        }
    }
}

DEV void chain_v(const Args& a, lptr lds, int h, int wid, int lane, int tid) {
    char* ws = a.ws;
    u32* flags = (u32*)(ws + OFF_FLAGS);
    const rsrc_t rws = __builtin_amdgcn_make_buffer_rsrc(ws, 0, WS_USED, 0x00020000);
    const u32x4 rs = mkrsrc(ws, WS_USED);
    const int g = lane >> 4, fr = lane & 15;
    bool dead = false;
    u32x8 Wv[10], Wo[5][2];
    {
        const u32x4 rv = mkrsrc(a.Wv, WQ_BYTES), ro = mkrsrc(a.Wo, WQ_BYTES);
        auto stv = [&](int pc) { stage_rows(rv, (u32)pc * 320u * 8192u + h * 256, 8192, 256, 320, lds + (pc & 1) * 81920, wid, lane); };
        auto sto = [&](int pc) { stage_rows(ro, (u32)(h * 128 + pc * 64) * 1280u, 1280, 1280, 64, lds + (pc & 1) * 81920, wid, lane); };
        stv(0); stv(1);
#pragma unroll
        for (int pc = 0; pc < 2; ++pc) {
            WAIT_V(10);
            RAW_BARRIER();
#pragma unroll
            for (int c = 0; c < 5; ++c) Wv[pc * 5 + c] = wfrag(lds + pc * 81920, 256, c * 64, wid * 16, lane);
            RAW_BARRIER();
            sto(pc);
        }
#pragma unroll
        for (int pc = 0; pc < 2; ++pc) {
            if (pc == 0) WAIT_V(10); else WAIT_V(0);
            RAW_BARRIER();
#pragma unroll
            for (int t = 0; t < 5; ++t) Wo[t][pc] = wfrag(lds + pc * 81920, 1280, 0, wid * 80 + t * 16, lane);
        }
        RAW_BARRIER();
    }
    const int grp = h >> 4, j = h & 15;
    const int ntile = j < 8 ? 3 : 2, tile0 = j < 8 ? 3 * j : 24 + 2 * (j - 8);
    const u32 ldsb = (u32)(size_t)lds;
    auto dma = [&](int seg) {
        const u32 d = ldsb + L_RING + (seg & 1) * 49152;
        const u32 sb = OFF_B + ((u32)(seg * 32 + grp * 16) * 40u + tile0) * 1024u;
        for (int t = 0; t < ntile; ++t)
#pragma unroll
            for (int hh = 0; hh < 16; ++hh) bdma<1>(rs, lane * 16, sb + (hh * 40 + t) * 1024, d + (t * 16 + hh) * 1024);
    };
    if (wid == 7) { wait_flag(flags, F_VB + grp, 16, dead); dma(0); }
    const int idx = (fr < 8) ? 0x4444 : 0xEEEE;
    const int ao = a_off(lane);
    for (int s = 0; s < NSEG; ++s) {
        if (wid == 0) TS(s, 0);
        gather_mem(a, rs, lds, s, wid, lane, tid, dead);
        if (wid == 0) TS(s, 1);
        RAW_BARRIER();
        if (wid == 0) TS(s, 2);
        {
            f32x4 c0 = {0.f, 0.f, 0.f, 0.f};
#pragma unroll
            for (int c = 0; c < 10; ++c) {
                const u32x4 av = cat(trread(lds + L_MEMI + c * 1024 + ao), trread(lds + L_MEMI + c * 1024 + (ao ^ 128)));
                c0 = smfmac(av, Wv[c], c0, idx);
            }
            const u32 d = pack2(fold32(c0[0], c0[2]), fold32(c0[1], c0[3]));
            *(LDSP u32*)(lds + L_QKI + memi_unit(wid * 16 + fr) * 16 + (2 * (g & 1) + (g >> 1)) * 4) = d;
        }
        if (wid == 7) { WAIT_V(0); TS(s, 9); }
        RAW_BARRIER();
        if (wid == 0) TS(s, 3);
        {
            f32x4 u[5];
#pragma unroll
            for (int t = 0; t < 5; ++t) u[t] = (f32x4){0.f, 0.f, 0.f, 0.f};
#pragma unroll
            for (int c = 0; c < 2; ++c) {
                const u32x4 av = cat(trread(lds + L_QKI + c * 1024 + ao), trread(lds + L_QKI + c * 1024 + (ao ^ 128)));
#pragma unroll
                for (int t = 0; t < 5; ++t) u[t] = smfmac(av, Wo[t][c], u[t], idx);
            }
            u32 x0 = pack2(fold32(u[0][0], u[1][0]), fold32(u[0][1], u[1][1])), x1 = pack2(fold32(u[0][2], u[1][2]), fold32(u[0][3], u[1][3]));
            u32 y0 = pack2(fold32(u[2][0], u[3][0]), fold32(u[2][1], u[3][1])), y1 = pack2(fold32(u[2][2], u[3][2]), fold32(u[2][3], u[3][3]));
            swap16(x0, y0); swap16(x1, y1);
            const u32 ub = OFF_U + (u32)(s * 32 + h) * 10240u + wid * 1280;
            bst128<16>(rws, (u32x4){nosent(x0), nosent(x1), nosent(y0), nosent(y1)}, ub + (((g & 1) * 2 + (g >> 1)) * 16 + fr) * 16);
            const u32 d = pack2(fold32(u[4][0], u[4][2]), fold32(u[4][1], u[4][3]));
            bst32<16>(rws, nosent(d), ub + (64 + fr) * 16 + (2 * (g & 1) + (g >> 1)) * 4);
        }
        if (wid == 0) TS(s, 4);
        if (s + 1 < NSEG) {
            if (wid < ntile) {
                u32x4 yb[NPRE], yu[4];
                lptr rg = lds + L_RING + (s & 1) * 49152 + wid * 16384 + lane * 16;
#pragma unroll
                for (int hh = 0; hh < NPRE; ++hh) yb[hh] = lds128(rg + hh * 1024);
                const u32 vo = (u32)g * 10240u + ((tile0 + wid) * 16 + fr) * 16;
                const u32 so = OFF_U + (u32)(s * 32 + grp * 16) * 10240u;
                int spins = 0;
                for (;;) {
                    ld4v(rs, vo, vo + 4 * 10240, vo + 8 * 10240, vo + 12 * 10240, so, yu);
                    const u32 mx = umax(umax(max4(yu[0]), max4(yu[1])), umax(max4(yu[2]), max4(yu[3])));
                    if (__all(mx != SENT) || dead) break;
                    if (++spins > SPIN_CAP) dead = true;
                }
                if (wid == 0) TS(s, 6);
                RAW_BARRIER();
                if (wid == 0) TS(s, 7);
                f32x4 c0 = {0.f, 0.f, 0.f, 0.f};
#pragma unroll
                for (int c = 0; c < NPRE / 2; ++c) c0 = smfmac(lds128(lds + L_PI + c * 1024 + lane * 16), cat(yb[2 * c], yb[2 * c + 1]), c0, idx);
#pragma unroll
                for (int c = 0; c < 2; ++c) {
                    lptr pm = lds + L_PI + 8192 + c * 1024 + g * 256 + fr * 8;
                    c0 = smfmac(cat(*(LDSP u32x2*)pm, *(LDSP u32x2*)(pm + 128)), cat(yu[2 * c], yu[2 * c + 1]), c0, idx);
                }
#pragma unroll
                for (int c = NPRE / 2; c < 8; ++c)
                    c0 = smfmac(lds128(lds + L_PI + c * 1024 + lane * 16), cat(lds128(rg + 2 * c * 1024), lds128(rg + 2 * c * 1024 + 1024)), c0, idx);
                u32 e0 = pack2(fold32(c0[0], c0[2]), fold32(c0[1], c0[3])), e1 = e0;
                swap32(e0, e1);
                u32 f0 = e0, f1 = e1;
                swap16(e0, f0); swap16(e1, f1);
                if (g == 0) bst128<16>(rws, (u32x4){nosent(e0), nosent(e1), nosent(f0), nosent(f1)},
                                       OFF_MX + (u32)((s + 1) * 2 + grp) * 10240u + ((tile0 + wid) * 16 + fr) * 16);
            } else {
                if (wid >= 3 && wid < 7) {
                    const int c0 = wid < 5 ? (wid - 3) * 3 : 6 + (wid - 5) * 2, c2 = wid < 5 ? c0 + 2 : c0 + 1;
                    const u32 so = OFF_P + (u32)(s * 2 + grp) * 10240u;
                    u32x4 t[3];
                    int spins = 0;
                    for (;;) {
                        ld3v(rs, c0 * 1024 + lane * 16, c2 * 1024 + lane * 16, so, t);
                        const u32 mx = umax(umax(max4(t[0]), max4(t[1])), max4(t[2]));
                        if (__all(mx != SENT) || dead) break;
                        if (++spins > SPIN_CAP) dead = true;
                    }
                    *(LDSP u32x4*)(lds + L_PI + c0 * 1024 + lane * 16) = t[0];
                    *(LDSP u32x4*)(lds + L_PI + c0 * 1024 + 1024 + lane * 16) = t[1];
                    *(LDSP u32x4*)(lds + L_PI + c2 * 1024 + lane * 16) = t[2];
                    if (wid == 3) TS(s, 8);
                } else if (wid == 7 && s + 1 < NSEG - 1) {
                    if (((s + 1) & 3) == 0) wait_flag(flags, F_VB + ((s + 1) >> 2) * 2 + grp, 16, dead);
                    dma(s + 1);
                    if (ntile == 3) WAIT_V(48); else WAIT_V(32);
                } else {
                    WAIT_V(0);
                }
                RAW_BARRIER();
                if (wid == 7 && lane == 0) flag_add(flags, F_X + s);
            }
        }
    }
    WAIT_V(0);
    RAW_BARRIER();
    if (tid == 0) flag_add(flags, F_X + NSEG - 1);
}

__global__ void __launch_bounds__(512, 2) main_kernel(Args a) {
    __shared__ __attribute__((aligned(1024))) char smem[163840];
    lptr lds = (lptr)smem;
    const int tid = threadIdx.x, lane = tid & 63, wid = __builtin_amdgcn_readfirstlane(tid >> 6);
    const int xcd = blockIdx.x & 7, slot = blockIdx.x >> 3;
    if (xcd >= 2) worker(a, lds, (LDSP int*)(lds + 163824), wid, lane, tid);
    else if (slot < 16) chain_s(a, lds, xcd * 16 + slot, wid, lane, tid);
    else chain_v(a, lds, xcd * 16 + slot - 16, wid, lane, tid);
}

extern "C" void kernel_launch(void* const* d_in, const int* in_sizes, int n_in, void* d_out, int out_size, void* d_ws, size_t ws_size,
                              hipStream_t stream) {
    Args a;
    a.x = (const char*)d_in[0]; a.mem0 = (const char*)d_in[1]; a.Wq = (const char*)d_in[2]; a.Wk = (const char*)d_in[3];
    a.Wv = (const char*)d_in[4]; a.Wo = (const char*)d_in[5]; a.w1 = (const char*)d_in[8]; a.b1 = (const char*)d_in[9];
    a.w2 = (const char*)d_in[10]; a.b2 = (const char*)d_in[11];
    a.out = (char*)d_out; a.ws = (char*)d_ws;
    prologue_kernel<<<512, 256, 0, stream>>>((const u32*)d_in[0], (char*)d_ws);
    main_kernel<<<256, 512, 0, stream>>>(a);
}
```

```cpp
#include <hip/hip_runtime.h>
#include <hip/hip_bf16.h>
#include <stdint.h>
#pragma clang diagnostic ignored "-Winline-asm"

typedef unsigned u32;
typedef u32 u32x2 __attribute__((ext_vector_type(2)));
typedef u32 u32x4 __attribute__((ext_vector_type(4)));
typedef u32 u32x8 __attribute__((ext_vector_type(8)));
typedef float f32x4 __attribute__((ext_vector_type(4)));
typedef __bf16 bf16x2 __attribute__((ext_vector_type(2)));
typedef __bf16 bf16x8 __attribute__((ext_vector_type(8)));
typedef __bf16 bf16x16 __attribute__((ext_vector_type(16)));
typedef short s16x4 __attribute__((ext_vector_type(4)));
#define LDSP __attribute__((address_space(3)))
typedef LDSP char* lptr;
#define DEV __device__ __forceinline__

constexpr int SEQ = 2048, DM = 640, NH = 32, DH = 128, NSEG = 64, FF = 1728;
constexpr u32 SENT = 0xFFFFFFFFu;
constexpr u32 WQ_BYTES = 640u * 4096u * 2u, W1_BYTES = 640u * 1728u * 2u;
constexpr int SPIN_CAP = 400000;

constexpr u32 MB = 1u << 20;
constexpr u32 OFF_FLAGS = 0;
constexpr u32 FLAG_BYTES = 128 * 1024;
constexpr u32 OFF_MX = 2 * MB;
constexpr u32 OFF_P = 6 * MB;
constexpr u32 OFF_U = 8 * MB;
constexpr u32 SENT_END = OFF_U + 64u * 32u * 10240u;
constexpr u32 OFF_XN = 32 * MB;
constexpr u32 OFF_G = 36 * MB;
constexpr u32 OFF_H = 40 * MB;
constexpr u32 OFF_PX = 48 * MB;
constexpr u32 OFF_PXM = 54 * MB;
constexpr u32 OFF_Q = 56 * MB;
constexpr u32 OFF_K = 76 * MB;
constexpr u32 OFF_B = 96 * MB;
constexpr u32 WS_USED = 176 * MB;
constexpr int F_QX = 0, F_QG = 8, F_VB = 16  , F_H = 48  , F_G = 64  , F_X = 96  , F_KQ = 256  ;

#define TS(slot, k) do {} while (0)
#define TSV(slot, k, v) do {} while (0)
#define WAIT_V(n) asm volatile("s_waitcnt vmcnt(%0)" ::"n"(n) : "memory")
#define WAIT_L0() asm volatile("s_waitcnt lgkmcnt(0)" ::: "memory")
#define RAW_BARRIER() do { WAIT_L0(); __builtin_amdgcn_s_barrier(); asm volatile("" ::: "memory"); } while (0)

DEV u32 pack2(float a, float b) { bf16x2 v = {(__bf16)a, (__bf16)b}; return __builtin_bit_cast(u32, v); }
DEV float bfr(float a) { return (float)(__bf16)a; }
DEV float blo(u32 d) { return __builtin_bit_cast(float, d << 16); }
DEV float bhi(u32 d) { return __builtin_bit_cast(float, d & 0xffff0000u); }
DEV u32 fbits(float f) { return __builtin_bit_cast(u32, f); }
DEV float bitsf(u32 u) { return __builtin_bit_cast(float, u); }
DEV u32 nosent(u32 d) { return d < 0xFFFFFFFEu ? d : 0xFFFFFFFEu; }
DEV void swap32(u32& a, u32& b) { auto r = __builtin_amdgcn_permlane32_swap(a, b, false, false); a = r[0]; b = r[1]; }
DEV void swap16(u32& a, u32& b) { auto r = __builtin_amdgcn_permlane16_swap(a, b, false, false); a = r[0]; b = r[1]; }
DEV float fold32(float a, float b) { u32 x = fbits(a), y = fbits(b); swap32(x, y); return bitsf(x) + bitsf(y); }
DEV float xmax(float v) {
    u32 a = fbits(v), b = a; swap32(a, b); v = fmaxf(bitsf(a), bitsf(b));
    a = fbits(v); b = a; swap16(a, b); return fmaxf(bitsf(a), bitsf(b));
}
DEV float xsum(float v) {
    u32 a = fbits(v), b = a; swap32(a, b); v = bitsf(a) + bitsf(b);
    a = fbits(v); b = a; swap16(a, b); return bitsf(a) + bitsf(b);
}
DEV u32x2 trread(lptr p) { s16x4 v = __builtin_amdgcn_ds_read_tr16_b64_v4i16((LDSP s16x4*)p); return __builtin_bit_cast(u32x2, v); }
DEV u32x4 lds128(lptr p) { return *(LDSP u32x4*)p; }
DEV u32x4 cat(u32x2 a, u32x2 b) { return (u32x4){a.x, a.y, b.x, b.y}; }
DEV u32x8 cat(u32x4 a, u32x4 b) { return (u32x8){a.x, a.y, a.z, a.w, b.x, b.y, b.z, b.w}; }
DEV f32x4 mfma(u32x4 a, u32x4 b, f32x4 c) {
    return __builtin_amdgcn_mfma_f32_16x16x32_bf16(__builtin_bit_cast(bf16x8, a), __builtin_bit_cast(bf16x8, b), c, 0, 0, 0);
}
DEV f32x4 smfmac(u32x4 a, u32x8 b, f32x4 c, int idx) {
    return __builtin_amdgcn_smfmac_f32_16x16x64_bf16(__builtin_bit_cast(bf16x8, a), __builtin_bit_cast(bf16x16, b), c, idx, 0, 0);
}
template <int SC1> DEV void bdma(u32x4 rs, u32 vo, u32 so, u32 lo) {
    if (SC1) asm volatile("s_mov_b32 m0, %3\n\ts_nop 3\n\tbuffer_load_dwordx4 %0, %1, %2 offen sc1 lds" ::"v"(vo), "s"(rs), "s"(so), "s"(lo) : "memory", "m0");
    else asm volatile("s_mov_b32 m0, %3\n\ts_nop 3\n\tbuffer_load_dwordx4 %0, %1, %2 offen lds" ::"v"(vo), "s"(rs), "s"(so), "s"(lo) : "memory", "m0");
}
DEV u32x4 mkrsrc(const void* p, u32 bytes) {
    unsigned long long a = (unsigned long long)p;
    u32x4 r = {(u32)a, (u32)(a >> 32) & 0xffffu, bytes, 0x00020000u};
    r.x = __builtin_amdgcn_readfirstlane(r.x); r.y = __builtin_amdgcn_readfirstlane(r.y);
    r.z = __builtin_amdgcn_readfirstlane(r.z); r.w = __builtin_amdgcn_readfirstlane(r.w);
    return r;
}
DEV u32 max4(u32x4 v) { u32 a = v.x > v.y ? v.x : v.y, b = v.z > v.w ? v.z : v.w; return a > b ? a : b; }
DEV u32 umax(u32 a, u32 b) { return a > b ? a : b; }

typedef __amdgpu_buffer_rsrc_t rsrc_t;
template <int AUX> DEV void bst128(rsrc_t r, u32x4 v, u32 off) { __builtin_amdgcn_raw_buffer_store_b128(v, r, off, 0, AUX); }
template <int AUX> DEV void bst64(rsrc_t r, u32x2 v, u32 off) { __builtin_amdgcn_raw_buffer_store_b64(v, r, off, 0, AUX); }
template <int AUX> DEV void bst32(rsrc_t r, u32 v, u32 off) { __builtin_amdgcn_raw_buffer_store_b32(v, r, off, 0, AUX); }
DEV u32x4 bld128s(rsrc_t r, u32 off) { return __builtin_amdgcn_raw_buffer_load_b128(r, off, 0, 16); }

DEV void wait_flag(u32* flags, int idx, u32 target, bool& dead) {
    if (dead) return;
    int spins = 0;
    for (;;) {
        u32 v = __hip_atomic_load(flags + idx * 32, __ATOMIC_RELAXED, __HIP_MEMORY_SCOPE_AGENT);
        if (__builtin_amdgcn_readfirstlane(v) >= target) break;
        if (++spins > SPIN_CAP) { dead = true; break; }
        __builtin_amdgcn_s_sleep(8);
    }
    asm volatile("" ::: "memory");
}
DEV void flag_add(u32* flags, int idx) { __hip_atomic_fetch_add(flags + idx * 32, 1u, __ATOMIC_RELAXED, __HIP_MEMORY_SCOPE_AGENT); }

__global__ void __launch_bounds__(256) prologue_kernel(const u32* __restrict__ x, char* __restrict__ ws) {
    const int tid = threadIdx.x, lane = tid & 63, wid = tid >> 6;
    const int row = blockIdx.x * 4 + wid;
    u32 d[5];
#pragma unroll
    for (int i = 0; i < 5; ++i) d[i] = x[row * 320 + i * 64 + lane];
    {
        const u32x4 s = {SENT, SENT, SENT, SENT};
        constexpr u32 n16 = (SENT_END - OFF_MX) / 16;
        u32x4* p = (u32x4*)(ws + OFF_MX);
        for (u32 i = blockIdx.x * 256 + tid; i < n16; i += 512 * 256) p[i] = s;
        const u32x4 z = {0, 0, 0, 0};
        u32 j = blockIdx.x * 256 + tid;
        if (j < FLAG_BYTES / 16) ((u32x4*)(ws + OFF_FLAGS))[j] = z;
    }
    float v[10], s = 0.f;
#pragma unroll
    for (int i = 0; i < 5; ++i) { v[2 * i] = blo(d[i]); v[2 * i + 1] = bhi(d[i]); s += v[2 * i] + v[2 * i + 1]; }
#pragma unroll
    for (int o = 32; o; o >>= 1) s += __shfl_xor(s, o);
    const float mean = s * (1.0f / DM);
    float q = 0.f;
#pragma unroll
    for (int i = 0; i < 10; ++i) { float t = v[i] - mean; q += t * t; }
#pragma unroll
    for (int o = 32; o; o >>= 1) q += __shfl_xor(q, o);
    const float mb = bfr(mean), vb = bfr(q * (1.0f / DM));
    const float den = bfr(sqrtf(bfr(vb + 1e-5f)));
    u32* xn = (u32*)(ws + OFF_XN);
#pragma unroll
    for (int i = 0; i < 5; ++i) xn[row * 320 + i * 64 + lane] = pack2(bfr(v[2 * i] - mb) / den, bfr(v[2 * i + 1] - mb) / den);
}

template <int NT>
DEV void gemm_core(f32x4 (&acc)[4][NT], u32x4 rX, u32 xoff, int ldx, u32x4 rW0, u32x4 rW1, u32 woff0, u32 woff1, int ldw, int chmax1, int nkt,
                   lptr lds, int wid, int lane) {
    constexpr int NP = NT / 2, STAGE = 16384 * (1 + NP), G = 2 + 2 * NP;
    const int sb = lane * 16, swz = sb ^ (((sb >> 9) & 1) << 5);
    const u32 xv = (u32)(16 * wid + (swz >> 6)) * ldx + (swz & 63);
    u32 wv0[2], wv1[2];
#pragma unroll
    for (int i = 0; i < 2; ++i) {
        const int row = 8 * wid + 4 * i + (lane >> 4);
        const int ch = (lane & 15) ^ (((row & 3) << 2) | ((row >> 2) & 3));
        wv0[i] = (u32)row * ldw + ch * 16;
        wv1[i] = (u32)row * ldw + (ch < chmax1 ? ch : chmax1) * 16;
    }
    const u32 ldsb = (u32)(size_t)lds;
    auto issue = [&](int kt, int buf) {
        const u32 b = ldsb + buf * STAGE;
        bdma<1>(rX, xv, xoff + kt * 128, b + wid * 2048);
        bdma<1>(rX, xv, xoff + kt * 128 + 64, b + wid * 2048 + 1024);
        const u32 wk = (u32)kt * 64u * ldw;
#pragma unroll
        for (int i = 0; i < 2; ++i) {
            bdma<0>(rW0, wv0[i], woff0 + wk, b + 16384 + (8 * wid + 4 * i) * 256);
            if (NP == 2) bdma<0>(rW1, wv1[i], woff1 + wk, b + 32768 + (8 * wid + 4 * i) * 256);
        }
    };
    const int wr = wid >> 2, wc = wid & 3, g = lane >> 4, q = (lane >> 2) & 3, p = lane & 3;
    const int ob = (lane & 15) * 64 + g * 16;
    const int xo = wr * 8192 + (ob ^ (((ob >> 9) & 1) << 5));
    int wo[NT][2];
#pragma unroll
    for (int n = 0; n < NT; ++n)
#pragma unroll
        for (int t = 0; t < 2; ++t) {
            const int panel = (NT == 4) ? (wc >> 1) : 0;
            const int c = (NT == 4) ? ((wc & 1) * 4 + n) : (wc * 2 + n);
            wo[n][t] = 16384 * (1 + panel) + 256 * (8 * g + 4 * t + q) + 16 * ((2 * c + (p >> 1)) ^ ((q << 2) | ((2 * g + t) & 3))) + 8 * (p & 1);
        }
#pragma unroll
    for (int m = 0; m < 4; ++m)
#pragma unroll
        for (int n = 0; n < NT; ++n) acc[m][n] = (f32x4){0.f, 0.f, 0.f, 0.f};
    issue(0, 0);
    issue(1, 1);
    int buf = 0;
    for (int kt = 0; kt < nkt; ++kt) {
        if (kt + 1 < nkt) WAIT_V(G); else WAIT_V(0);
        RAW_BARRIER();
        if (kt + 2 < nkt) issue(kt + 2, buf >= 1 ? buf - 1 : 2);
        lptr b = lds + buf * STAGE;
#pragma unroll
        for (int ks = 0; ks < 2; ++ks) {
            u32x4 xf[4], wf[NT];
#pragma unroll
            for (int m = 0; m < 4; ++m) xf[m] = lds128(b + xo + (m * 2 + ks) * 1024);
#pragma unroll
            for (int n = 0; n < NT; ++n) wf[n] = cat(trread(b + wo[n][0] + ks * 8192), trread(b + wo[n][1] + ks * 8192));
#pragma unroll
            for (int m = 0; m < 4; ++m)
#pragma unroll
                for (int n = 0; n < NT; ++n) acc[m][n] = mfma(wf[n], xf[m], acc[m][n]);
        }
        buf = (buf == 2) ? 0 : buf + 1;
    }
}
DEV u32x4 pair8(u32 a0, u32 a1, u32 b0, u32 b1) { swap16(a0, b0); swap16(a1, b1); return (u32x4){a0, a1, b0, b1}; }

struct Args {
    const char *x, *mem0, *Wq, *Wk, *Wv, *Wo, *w1, *b1, *w2, *b2;
    char *out, *ws;
};

DEV void worker(const Args& a, lptr lds, LDSP int* bc, int wid, int lane, int tid) {
    char* ws = a.ws;
    u32* flags = (u32*)(ws + OFF_FLAGS);
    const rsrc_t rws = __builtin_amdgcn_make_buffer_rsrc(ws, 0, WS_USED, 0x00020000);
    const u32x4 rs = mkrsrc(ws, WS_USED);
    const u32 ldsb = (u32)(size_t)lds;
    const int xq = blockIdx.x & 7;
    const int wr = wid >> 2, wc = wid & 3, g = lane >> 4, fr = lane & 15;
    const int c8 = 8 * ((g >> 1) + 2 * (g & 1));
    int qi = 0;
    bool dead = false;
    int nit = 0;
    for (;;) {
        if (wid == 0 && nit < 80) TS(nit, 0);
        if (tid == 0) {
            int type = -1, p0 = 0, p1 = 0;
            while (qi < 8) {
                const int qq = (xq + qi) & 7;
                const int it = (int)__hip_atomic_fetch_add(flags + (F_QX + qq) * 32, 1u, __ATOMIC_RELAXED, __HIP_MEMORY_SCOPE_AGENT);
                if (it < 128) { type = (it & 1) ^ 1; p0 = it >> 3; p1 = qq + 8 * ((it >> 1) & 3); break; }
                ++qi;
            }
            if (type < 0) {
                const int it = (int)__hip_atomic_fetch_add(flags + F_QG * 32, 1u, __ATOMIC_RELAXED, __HIP_MEMORY_SCOPE_AGENT);
                if (it < 112) { type = 2; p0 = it / 7; p1 = it % 7; }
                else if (it < 192) { type = 3; p0 = (it - 112) / 5; p1 = (it - 112) % 5; }
                else if (it < 192 + 640) { type = 4; p0 = (it - 192) / 10; p1 = (it - 192) % 10; }
            }
            bc[0] = type; bc[1] = p0; bc[2] = p1;
        }
        RAW_BARRIER();
        const int type = __builtin_amdgcn_readfirstlane(bc[0]), p0 = __builtin_amdgcn_readfirstlane(bc[1]), p1 = __builtin_amdgcn_readfirstlane(bc[2]);
        RAW_BARRIER();
        if (type < 0) break;
        if (wid == 0 && nit < 80) { TS(nit, 1); TSV(nit, 2, type * 65536 + p0 * 256 + p1 + 1); }
        ++nit;
        if (type == 0 || type == 2) {
            f32x4 acc[4][4];
            {
                const bool qk = type == 0;
                const u32x4 r0 = mkrsrc(qk ? a.Wq : a.w1, qk ? WQ_BYTES : W1_BYTES), r1 = mkrsrc(qk ? a.Wk : a.w1, qk ? WQ_BYTES : W1_BYTES);
                const u32 o0 = qk ? p1 * 256 : p1 * 512, o1 = qk ? o0 : o0 + 256;
                gemm_core<4>(acc, rs, OFF_XN + (u32)p0 * 128u * 1280u, 1280, r0, r1, o0, o1, qk ? 8192 : FF * 2, (!qk && p1 == 6) ? 7 : 15, 10, lds, wid, lane);
            }
            if (type == 0) {
                const u32 base = ((wc >> 1) ? OFF_K : OFF_Q) + (u32)(p0 * 128 + wr * 64 + fr) * 8192u + p1 * 256 + ((wc & 1) * 64 + c8) * 2;
#pragma unroll
                for (int m = 0; m < 4; ++m)
#pragma unroll
                    for (int n = 0; n < 4; n += 2) {
                        u32x4 v = pair8(pack2(acc[m][n][0], acc[m][n][1]), pack2(acc[m][n][2], acc[m][n][3]),
                                        pack2(acc[m][n + 1][0], acc[m][n + 1][1]), pack2(acc[m][n + 1][2], acc[m][n + 1][3]));
                        bst128<16>(rws, v, base + m * 16 * 8192 + n * 32);
                    }
            } else {
                const int col0 = p1 * 256 + wc * 64;
                u32x2 bb[4];
#pragma unroll
                for (int n = 0; n < 4; ++n) { int c = col0 + n * 16 + 4 * g; c = c < FF - 4 ? c : FF - 4; bb[n] = *(const u32x2*)(a.b1 + c * 2); }
                const u32 base = OFF_H + (u32)(p0 * 128 + wr * 64 + fr) * (FF * 2) + (col0 + c8) * 2;
#pragma unroll
                for (int m = 0; m < 4; ++m) {
                    u32 h[4][2];
#pragma unroll
                    for (int n = 0; n < 4; ++n) {
                        float t0 = fmaxf(bfr(bfr(acc[m][n][0]) + blo(bb[n].x)), 0.f), t1 = fmaxf(bfr(bfr(acc[m][n][1]) + bhi(bb[n].x)), 0.f);
                        float t2 = fmaxf(bfr(bfr(acc[m][n][2]) + blo(bb[n].y)), 0.f), t3 = fmaxf(bfr(bfr(acc[m][n][3]) + bhi(bb[n].y)), 0.f);
                        h[n][0] = pack2(t0 * t0, t1 * t1); h[n][1] = pack2(t2 * t2, t3 * t3);
                    }
#pragma unroll
                    for (int n = 0; n < 4; n += 2) {
                        u32x4 v = pair8(h[n][0], h[n][1], h[n + 1][0], h[n + 1][1]);
                        if (col0 < FF) bst128<16>(rws, v, base + m * 16 * (FF * 2) + n * 32);
                    }
                }
            }
            WAIT_V(0);
            RAW_BARRIER();
            if (tid == 0) flag_add(flags, type == 0 ? F_KQ + p0 * 32 + p1 : F_H + p0);
        } else if (type == 1 || type == 3) {
            f32x4 acc[4][2];
            u32 wop[4];
            const u32x4 rWo = mkrsrc(a.Wo, WQ_BYTES);
            const u32 wob = (u32)p1 * 128u * 1280u;
            if (type == 1) {
#pragma unroll
                for (int i = 0; i < 4; ++i) {
                    const int row = 16 * wid + 4 * i + g;
                    const int ch = fr ^ (((row & 3) << 2) | ((row >> 2) & 3));
                    wop[i] = (u32)row * 1280u + ch * 16;
                    bdma<0>(rWo, wop[i], wob, ldsb + 3 * 32768 + (16 * wid + 4 * i) * 256);
                }
            } else {
                wait_flag(flags, F_H + p0, 7, dead);
            }
            {
                const bool v = type == 1;
                const u32x4 r0 = mkrsrc(v ? a.Wv : a.w2, v ? WQ_BYTES : W1_BYTES);
                gemm_core<2>(acc, rs, v ? OFF_XN + (u32)p0 * 128u * 1280u : OFF_H + (u32)p0 * 128u * (FF * 2), v ? 1280 : FF * 2, r0, r0, p1 * 256, 0,
                             v ? 8192 : 1280, 15, v ? 10 : 27, lds, wid, lane);
            }
            if (type == 1) {
                {
                    const int ob = fr * 64 + c8 * 2;
                    lptr vi = lds + 4 * 32768 + wc * 1024 + (ob ^ (((ob >> 9) & 1) << 5));
#pragma unroll
                    for (int m = 0; m < 4; ++m)
                        *(LDSP u32x4*)(vi + (wr * 4 + m) * 4096) = pair8(pack2(acc[m][0][0], acc[m][0][1]), pack2(acc[m][0][2], acc[m][0][3]),
                                                                          pack2(acc[m][1][0], acc[m][1][1]), pack2(acc[m][1][2], acc[m][1][3]));
                }
                RAW_BARRIER();
#pragma unroll
                for (int pn = 1; pn < 4; ++pn)
#pragma unroll
                    for (int i = 0; i < 4; ++i) bdma<0>(rWo, wop[i], wob + pn * 256, ldsb + (pn - 1) * 32768 + (16 * wid + 4 * i) * 256);
                u32x4 vf[4][4];
                {
                    const int ob = fr * 64 + g * 16;
                    lptr vi = lds + 4 * 32768 + wr * 16384 + (ob ^ (((ob >> 9) & 1) << 5));
#pragma unroll
                    for (int m = 0; m < 4; ++m)
#pragma unroll
                        for (int ks = 0; ks < 4; ++ks) vf[m][ks] = lds128(vi + m * 4096 + ks * 1024);
                }
                RAW_BARRIER();
#pragma unroll
                for (int i = 0; i < 4; ++i) bdma<0>(rWo, wop[i], wob + 4 * 256, ldsb + 4 * 32768 + (16 * wid + 4 * i) * 256);
                const int q = (lane >> 2) & 3, p = lane & 3;
                int wo[2][2];
#pragma unroll
                for (int n = 0; n < 2; ++n)
#pragma unroll
                    for (int t = 0; t < 2; ++t)
                        wo[n][t] = 256 * (8 * g + 4 * t + q) + 16 * ((2 * (wc * 2 + n) + (p >> 1)) ^ ((q << 2) | ((2 * g + t) & 3))) + 8 * (p & 1);
                for (int pn = 0; pn < 5; ++pn) {
                    if (pn) { WAIT_V(16); RAW_BARRIER(); }
                    lptr b = lds + (pn == 0 ? 3 : (pn == 4 ? 4 : pn - 1)) * 32768;
                    f32x4 c2[4][2];
#pragma unroll
                    for (int m = 0; m < 4; ++m) { c2[m][0] = (f32x4){0.f, 0.f, 0.f, 0.f}; c2[m][1] = c2[m][0]; }
#pragma unroll
                    for (int ks = 0; ks < 4; ++ks) {
                        u32x4 wf[2];
#pragma unroll
                        for (int n = 0; n < 2; ++n) wf[n] = cat(trread(b + wo[n][0] + ks * 8192), trread(b + wo[n][1] + ks * 8192));
#pragma unroll
                        for (int m = 0; m < 4; ++m)
#pragma unroll
                            for (int n = 0; n < 2; ++n) c2[m][n] = mfma(vf[m][ks], wf[n], c2[m][n]);
                    }
#pragma unroll
                    for (int sg = 0; sg < 2; ++sg)
#pragma unroll
                        for (int n = 0; n < 2; ++n) {
                            u32x4 v = {pack2(c2[2 * sg][n][0], c2[2 * sg][n][1]), pack2(c2[2 * sg][n][2], c2[2 * sg][n][3]),
                                       pack2(c2[2 * sg + 1][n][0], c2[2 * sg + 1][n][1]), pack2(c2[2 * sg + 1][n][2], c2[2 * sg + 1][n][3])};
                            const u32 seg = p0 * 4 + wr * 2 + sg;
                            bst128<16>(rws, v, OFF_B + ((seg * 32 + p1) * 40 + pn * 8 + wc * 2 + n) * 1024u + lane * 16);
                        }
                }
            } else {
                const int col0 = p1 * 128 + wc * 32;
                u32x2 bb[2];
#pragma unroll
                for (int n = 0; n < 2; ++n) bb[n] = *(const u32x2*)(a.b2 + (col0 + n * 16 + 4 * g) * 2);
                const u32 base = OFF_G + (u32)(p0 * 128 + wr * 64 + fr) * 1280u + (col0 + c8) * 2;
#pragma unroll
                for (int m = 0; m < 4; ++m) {
                    u32 h[2][2];
#pragma unroll
                    for (int n = 0; n < 2; ++n) {
                        float t[4] = {bfr(bfr(acc[m][n][0]) + blo(bb[n].x)), bfr(bfr(acc[m][n][1]) + bhi(bb[n].x)),
                                      bfr(bfr(acc[m][n][2]) + blo(bb[n].y)), bfr(bfr(acc[m][n][3]) + bhi(bb[n].y))};
#pragma unroll
                        for (int r = 0; r < 4; ++r) t[r] = t[r] / (1.0f + __expf(-t[r]));
                        h[n][0] = pack2(t[0], t[1]); h[n][1] = pack2(t[2], t[3]);
                    }
                    bst128<16>(rws, pair8(h[0][0], h[0][1], h[1][0], h[1][1]), base + m * 16 * 1280);
                }
            }
            WAIT_V(0);
            RAW_BARRIER();
            if (tid == 0) flag_add(flags, type == 1 ? F_VB + p0 * 2 + (p1 >> 4) : F_G + p0);
        } else {
            const int seg = p0, cg = p1;
            wait_flag(flags, F_VB + (seg >> 2) * 2 + (wid >> 2), 16, dead);
            u32x4 yb[4][4], yu[4], px[4][2], pm[2];
#pragma unroll
            for (int hh = 0; hh < 4; ++hh)
#pragma unroll
                for (int n = 0; n < 4; ++n) yb[hh][n] = bld128s(rws, OFF_B + ((seg * 32 + wid * 4 + hh) * 40 + cg * 4 + n) * 1024u + lane * 16);
            wait_flag(flags, F_G + (seg >> 2), 5, dead);
            const u32 eo = (u32)(seg * 32 + (wid >> 2) * 16 + fr) * 1280u + (cg * 64 + (wid & 3) * 16 + 4 * g) * 2;
            const u32x2 xv = *(const u32x2*)(a.x + eo);
            const u32x2 gv = __builtin_amdgcn_raw_buffer_load_b64(rws, OFF_G + eo, 0, 16);
            wait_flag(flags, F_X + seg, 96, dead);
#pragma unroll
            for (int hh = 0; hh < 4; ++hh)
#pragma unroll
                for (int qt = 0; qt < 2; ++qt) px[hh][qt] = bld128s(rws, OFF_PX + (seg * 32 + wid * 4 + hh) * 2048u + qt * 1024 + lane * 16);
#pragma unroll
            for (int qt = 0; qt < 2; ++qt) pm[qt] = bld128s(rws, OFF_PXM + (seg * 8 + wid) * 2048u + qt * 1024 + lane * 16);
#pragma unroll
            for (int n = 0; n < 4; ++n) yu[n] = bld128s(rws, OFF_U + (seg * 32 + wid * 4 + g) * 10240u + (cg * 64 + n * 16 + fr) * 16);
            f32x4 c[2][4];
#pragma unroll
            for (int qt = 0; qt < 2; ++qt)
#pragma unroll
                for (int n = 0; n < 4; ++n) {
                    f32x4 t = {0.f, 0.f, 0.f, 0.f};
#pragma unroll
                    for (int hh = 0; hh < 4; ++hh) t = mfma(yb[hh][n], px[hh][qt], t);
                    c[qt][n] = mfma(yu[n], pm[qt], t);
                }
#pragma unroll
            for (int qt = 0; qt < 2; ++qt)
#pragma unroll
                for (int n = 0; n < 4; ++n) *(LDSP f32x4*)(lds + (wid * 8 + qt * 4 + n) * 1024 + lane * 16) = c[qt][n];
            RAW_BARRIER();
            f32x4 s = {0.f, 0.f, 0.f, 0.f};
#pragma unroll
            for (int w = 0; w < 8; ++w) s += *(LDSP f32x4*)(lds + (w * 8 + wid) * 1024 + lane * 16);
            u32x2 o;
            o.x = pack2(blo(xv.x) + bfr(bfr(s[0]) * blo(gv.x)), bhi(xv.x) + bfr(bfr(s[1]) * bhi(gv.x)));
            o.y = pack2(blo(xv.y) + bfr(bfr(s[2]) * blo(gv.y)), bhi(xv.y) + bfr(bfr(s[3]) * bhi(gv.y)));
            *(u32x2*)(a.out + eo) = o;
        }
    }
}

#define BL4 "buffer_load_dwordx4 "
DEV void ld4(u32x4 rs, u32 vo, u32 s0, u32 s1, u32x4 (&t)[4]) {
    asm volatile("s_nop 4\n\t" BL4 "%0, %4, %5, %6 offen sc1\n\t" BL4 "%1, %4, %5, %6 offen offset:1024 sc1\n\t"
                 BL4 "%2, %4, %5, %7 offen sc1\n\t" BL4 "%3, %4, %5, %7 offen offset:1024 sc1\n\ts_waitcnt vmcnt(0)"
                 : "=&v"(t[0]), "=&v"(t[1]), "=&v"(t[2]), "=&v"(t[3]) : "v"(vo), "s"(rs), "s"(s0), "s"(s1) : "memory");
}
DEV void ld4v(u32x4 rs, u32 v0, u32 v1, u32 v2, u32 v3, u32 s0, u32x4 (&t)[4]) {
    asm volatile("s_nop 4\n\t" BL4 "%0, %4, %8, %9 offen sc1\n\t" BL4 "%1, %5, %8, %9 offen sc1\n\t"
                 BL4 "%2, %6, %8, %9 offen sc1\n\t" BL4 "%3, %7, %8, %9 offen sc1\n\ts_waitcnt vmcnt(0)"
                 : "=&v"(t[0]), "=&v"(t[1]), "=&v"(t[2]), "=&v"(t[3]) : "v"(v0), "v"(v1), "v"(v2), "v"(v3), "s"(rs), "s"(s0) : "memory");
}
DEV void ld3v(u32x4 rs, u32 v0, u32 v2, u32 s0, u32x4 (&t)[3]) {
    asm volatile("s_nop 4\n\t" BL4 "%0, %3, %5, %6 offen sc1\n\t" BL4 "%1, %3, %5, %6 offen offset:1024 sc1\n\t"
                 BL4 "%2, %4, %5, %6 offen sc1\n\ts_waitcnt vmcnt(0)"
                 : "=&v"(t[0]), "=&v"(t[1]), "=&v"(t[2]) : "v"(v0), "v"(v2), "s"(rs), "s"(s0) : "memory");
}
DEV u32 add2(u32 a, u32 b) { return pack2(blo(a) + blo(b), bhi(a) + bhi(b)); }
DEV u32x4 add8(u32x4 a, u32x4 b) { return (u32x4){add2(a.x, b.x), add2(a.y, b.y), add2(a.z, b.z), add2(a.w, b.w)}; }
DEV int memi_unit(int k) { return k ^ (((k >> 4) & 1) << 3); }

constexpr int NPRE = 8;
constexpr int L_MEMI = 0, L_QKI = 10240, L_PI = 16384, L_RING = 32768;

DEV void gather_mem(const Args& a, u32x4 rs, lptr lds, int s, int wid, int lane, int tid, bool& dead) {
    if (s == 0) {
        for (int k = tid; k < DM; k += 512) {
            const unsigned short* m = (const unsigned short*)a.mem0 + k;
            u32x4 v;
            v.x = m[0] | ((u32)m[DM] << 16); v.y = m[2 * DM] | ((u32)m[3 * DM] << 16);
            v.z = m[4 * DM] | ((u32)m[5 * DM] << 16); v.w = m[6 * DM] | ((u32)m[7 * DM] << 16);
            *(LDSP u32x4*)(lds + L_MEMI + memi_unit(k) * 16) = v;
        }
    } else if (wid < 5) {
        u32x4 t[4];
        const u32 s0 = OFF_MX + (u32)s * 20480u;
        int spins = 0;
        for (;;) {
            ld4(rs, wid * 2048 + lane * 16, s0, s0 + 10240, t);
            const u32 mx = umax(umax(max4(t[0]), max4(t[1])), umax(max4(t[2]), max4(t[3])));
            if (__all(mx != SENT) || dead) break;
            if (++spins > SPIN_CAP) dead = true;
        }
        const int u = memi_unit(wid * 128 + lane);
        *(LDSP u32x4*)(lds + L_MEMI + u * 16) = add8(t[0], t[2]);
        *(LDSP u32x4*)(lds + L_MEMI + u * 16 + 1024) = add8(t[1], t[3]);
    }
}
DEV int a_off(int lane) {
    const int G = lane >> 4, q = (lane >> 2) & 3, p = lane & 3;
    return 16 * memi_unit(16 * G + 4 * (q >> 1) + (q & 1) + 2 * (p >> 1)) + 8 * (p & 1);
}
DEV void stage_rows(u32x4 r, u32 off, int ld, int rowb, int rows, lptr dst, int wid, int lane) {
    const int total = rows * rowb / 1024;
    for (int i = wid; i < total; i += 8) {
        const int b = i * 1024 + lane * 16;
        bdma<0>(r, (u32)(b / rowb) * ld + (b % rowb), off, (u32)(size_t)dst + i * 1024);
    }
}
DEV u32x8 wfrag(lptr img, int rowb, int k0, int col0, int lane) {
    const int g = lane >> 4, q = (lane >> 2) & 3, p = lane & 3;
    lptr b = img + (k0 + 8 * g + q) * rowb + (col0 + 4 * p) * 2;
    return cat(cat(trread(b), trread(b + 4 * rowb)), cat(trread(b + 32 * rowb), trread(b + 36 * rowb)));
}

DEV void chain_s(const Args& a, lptr lds, int h, int wid, int lane, int tid) {
    char* ws = a.ws;
    u32* flags = (u32*)(ws + OFF_FLAGS);
    const rsrc_t rws = __builtin_amdgcn_make_buffer_rsrc(ws, 0, WS_USED, 0x00020000);
    const u32x4 rs = mkrsrc(ws, WS_USED);
    const int g = lane >> 4, fr = lane & 15;
    bool dead = false;
    u32x8 W[2][10];
    {
        const u32x4 rq = mkrsrc(a.Wq, WQ_BYTES), rk = mkrsrc(a.Wk, WQ_BYTES);
        auto st = [&](int pc) {
            lptr d = lds + (pc & 1) * 65536;
            stage_rows(rq, (u32)pc * 128u * 8192u + h * 256, 8192, 256, 128, d, wid, lane);
            stage_rows(rk, (u32)pc * 128u * 8192u + h * 256, 8192, 256, 128, d + 32768, wid, lane);
        };
        st(0);
#pragma unroll
        for (int pc = 0; pc < 5; ++pc) {
            if (pc + 1 < 5) { st(pc + 1); WAIT_V(8); } else WAIT_V(0);
            RAW_BARRIER();
            lptr img = lds + (pc & 1) * 65536 + (wid >> 2) * 32768;
#pragma unroll
            for (int c = 0; c < 2; ++c)
#pragma unroll
                for (int t = 0; t < 2; ++t) W[t][pc * 2 + c] = wfrag(img, 256, c * 64, (wid & 3) * 32 + t * 16, lane);
            RAW_BARRIER();
        }
    }
    const int key0 = 16 * ((fr & 3) >> 1) + 8 * (fr >> 3) + 2 * ((fr >> 2) & 1) + (fr & 1);
    const u32 ldsb = (u32)(size_t)lds;
    auto dma = [&](int seg) {
        const u32 d = ldsb + L_RING + (seg & 1) * 16384;
        const u32 kv = key0 * 8192 + g * 16, qv = fr * 8192 + g * 16;
        const u32 sb = (u32)seg * 32u * 8192u + h * 256;
#pragma unroll
        for (int t = 0; t < 2; ++t)
#pragma unroll
            for (int c = 0; c < 4; ++c) {
                bdma<1>(rs, kv, OFF_K + sb + t * 4 * 8192 + c * 64, d + (t * 4 + c) * 1024);
                bdma<1>(rs, qv, OFF_Q + sb + t * 16 * 8192 + c * 64, d + 8192 + (t * 4 + c) * 1024);
            }
    };
    if (wid == 7) { wait_flag(flags, F_KQ + h, 1, dead); dma(0); dma(1); }
    const int idx = (fr < 8) ? 0x4444 : 0xEEEE;
    const int ao = a_off(lane);
    const float cs = 0.08838834764831845f * 1.4426950408889634f;
    const int grp = h >> 4, hl = h & 15;
    for (int s = 0; s < NSEG; ++s) {
        if (wid == 0) { TS(s, 0); TSV(s, 15, __builtin_amdgcn_s_memtime()); }
        gather_mem(a, rs, lds, s, wid, lane, tid, dead);
        if (wid == 0) TS(s, 1);
        if (wid == 7) WAIT_V(0);
        RAW_BARRIER();
        if (wid == 0) TS(s, 2);
        f32x4 c0 = {0.f, 0.f, 0.f, 0.f}, c1 = c0;
#pragma unroll
        for (int c = 0; c < 10; ++c) {
            const u32x4 av = cat(trread(lds + L_MEMI + c * 1024 + ao), trread(lds + L_MEMI + c * 1024 + (ao ^ 128)));
            c0 = smfmac(av, W[0][c], c0, idx);
            c1 = smfmac(av, W[1][c], c1, idx);
        }
        {
            const u32 d0 = pack2(fold32(c0[0], c1[0]), fold32(c0[1], c1[1])), d1 = pack2(fold32(c0[2], c1[2]), fold32(c0[3], c1[3]));
            LDSP u32* qi = (LDSP u32*)(lds + L_QKI + (wid * 32 + (lane >> 5) * 16 + fr) * 16 + (g & 1) * 4);
            qi[0] = d0; qi[2] = d1;
        }
        RAW_BARRIER();
        if (wid == 0) TS(s, 3);
        if (wid < 3) {
            lptr rg = lds + L_RING + (s & 1) * 16384;
            lptr qk = lds + L_QKI + (8 * g + ((lane >> 2) & 3)) * 16 + 8 * (lane & 1);
            f32x4 st[3];
#pragma unroll
            for (int t = 0; t < 3; ++t) st[t] = (f32x4){0.f, 0.f, 0.f, 0.f};
#pragma unroll
            for (int c = 0; c < 4; ++c) {
                u32x4 qf;
                if (wid == 0) qf = cat(trread(qk + c * 512), trread(qk + c * 512 + 64));
                else qf = lds128(rg + 8192 + ((wid - 1) * 4 + c) * 1024 + lane * 16);
                const u32x4 km = cat(trread(qk + 2048 + c * 512), trread(qk + 2048 + c * 512 + 64));
                st[0] = mfma(lds128(rg + c * 1024 + lane * 16), qf, st[0]);
                st[1] = mfma(lds128(rg + (4 + c) * 1024 + lane * 16), qf, st[1]);
                st[2] = mfma(km, qf, st[2]);
            }
            const float NEG = -3.0e38f;
            if (wid) {
                const int tq = (wid - 1) * 16 + fr;
#pragma unroll
                for (int t = 0; t < 2; ++t)
#pragma unroll
                    for (int r = 0; r < 4; ++r) {
                        const int key = 16 * (r >> 1) + 8 * (g >> 1) + 4 * t + 2 * (g & 1) + (r & 1);
                        st[t][r] = key <= tq ? st[t][r] : NEG;
                    }
            }
            float m = fmaxf(fmaxf(fmaxf(st[0][0], st[0][1]), fmaxf(st[0][2], st[0][3])), fmaxf(fmaxf(st[1][0], st[1][1]), fmaxf(st[1][2], st[1][3])));
            m = fmaxf(m, fmaxf(fmaxf(st[2][0], st[2][1]), fmaxf(st[2][2], st[2][3])));
            m = xmax(m);
            const float mc = m * cs;
            float sum = 0.f;
#pragma unroll
            for (int t = 0; t < 3; ++t)
#pragma unroll
                for (int r = 0; r < 4; ++r) {
                    const float e = __builtin_amdgcn_exp2f(__builtin_fmaf(st[t][r], cs, -mc));
                    st[t][r] = e;
                    sum += (t == 2 && g >= 2) ? 0.f : e;
                }
            sum = xsum(sum);
            const float inv = 1.0f / sum;
            u32 d[3][2];
#pragma unroll
            for (int t = 0; t < 3; ++t) { d[t][0] = pack2(st[t][0] * inv, st[t][1] * inv); d[t][1] = pack2(st[t][2] * inv, st[t][3] * inv); }
            if (wid == 0) {
                if (s + 1 < NSEG) {
                    const int qi = 4 * ((fr >> 1) & 1) + 2 * ((fr >> 2) & 1) + (fr & 1);
                    u32 e0 = d[2][0], e1 = d[2][1], f0 = e0, f1 = e1;
                    swap16(e0, f0); swap16(e1, f1);
                    const bool xp = fr < 8;
                    const u32x4 v = {nosent(xp ? d[0][0] : e0), nosent(xp ? d[0][1] : e1), nosent(xp ? d[1][0] : f0), nosent(xp ? d[1][1] : f1)};
                    if (xp || g == 0) bst128<16>(rws, v, OFF_P + (u32)(s * 32 + h) * 640u + (xp ? (8 * g + qi) * 16 : 512 + qi * 16));
                    TS(s, 4);
                }
            } else {
                swap16(d[0][0], d[1][0]); swap16(d[0][1], d[1][1]);
                bst128<16>(rws, (u32x4){d[0][0], d[1][0], d[0][1], d[1][1]}, OFF_PX + (u32)(s * 32 + h) * 2048u + (wid - 1) * 1024 + lane * 16);
                u32 e0 = d[2][0], e1 = d[2][1];
                swap16(d[2][0], e0); swap16(d[2][1], e1);
                if (g == 0) bst128<16>(rws, (u32x4){d[2][0], e0, d[2][1], e1}, OFF_PXM + (u32)(s * 8 + (h >> 2)) * 2048u + (wid - 1) * 1024 + ((h & 3) * 16 + fr) * 16);
                if (s >= NSEG - 2) {
                    WAIT_V(0);
                    if (lane == 0) flag_add(flags, F_X + s);
                }
            }
        }
        if (wid == 3 && h == 0 && s >= 2 && lane == 0) __hip_atomic_store(flags + (F_X + s - 2) * 32, 96u, __ATOMIC_RELAXED, __HIP_MEMORY_SCOPE_AGENT);
        if (wid == 7 && s >= 1 && s + 1 < NSEG) {
            if (((s + 1) & 3) == 0) wait_flag(flags, F_KQ + ((s + 1) >> 2) * 32 + h, 1, dead);
            dma(s + 1);
        }
    }
}

DEV void chain_v(const Args& a, lptr lds, int h, int wid, int lane, int tid) {
    char* ws = a.ws;
    u32* flags = (u32*)(ws + OFF_FLAGS);
    const rsrc_t rws = __builtin_amdgcn_make_buffer_rsrc(ws, 0, WS_USED, 0x00020000);
    const u32x4 rs = mkrsrc(ws, WS_USED);
    const int g = lane >> 4, fr = lane & 15;
    bool dead = false;
    u32x8 Wv[10], Wo[5][2];
    {
        const u32x4 rv = mkrsrc(a.Wv, WQ_BYTES), ro = mkrsrc(a.Wo, WQ_BYTES);
        auto stv = [&](int pc) { stage_rows(rv, (u32)pc * 320u * 8192u + h * 256, 8192, 256, 320, lds + (pc & 1) * 81920, wid, lane); };
        auto sto = [&](int pc) { stage_rows(ro, (u32)(h * 128 + pc * 64) * 1280u, 1280, 1280, 64, lds + (pc & 1) * 81920, wid, lane); };
        stv(0); stv(1);
#pragma unroll
        for (int pc = 0; pc < 2; ++pc) {
            WAIT_V(10);
            RAW_BARRIER();
#pragma unroll
            for (int c = 0; c < 5; ++c) Wv[pc * 5 + c] = wfrag(lds + pc * 81920, 256, c * 64, wid * 16, lane);
            RAW_BARRIER();
            sto(pc);
        }
#pragma unroll
        for (int pc = 0; pc < 2; ++pc) {
            if (pc == 0) WAIT_V(10); else WAIT_V(0);
            RAW_BARRIER();
#pragma unroll
            for (int t = 0; t < 5; ++t) Wo[t][pc] = wfrag(lds + pc * 81920, 1280, 0, wid * 80 + t * 16, lane);
        }
        RAW_BARRIER();
    }
    const int grp = h >> 4, j = h & 15;
    const int ntile = j < 8 ? 3 : 2, tile0 = j < 8 ? 3 * j : 24 + 2 * (j - 8);
    const u32 ldsb = (u32)(size_t)lds;
    auto dma = [&](int seg) {
        const u32 d = ldsb + L_RING + (seg & 1) * 49152 + wid * 2048;
        const u32 sb = OFF_B + ((u32)(seg * 32 + grp * 16 + wid * 2) * 40u + tile0) * 1024u;
#pragma unroll
        for (int t = 0; t < 3; ++t)
            if (t < ntile) {
                bdma<1>(rs, lane * 16, sb + t * 1024, d + t * 16384);
                bdma<1>(rs, lane * 16, sb + (40 + t) * 1024, d + t * 16384 + 1024);
            }
    };
    wait_flag(flags, F_VB + grp, 16, dead);
    dma(0);
    WAIT_V(0);
    const int idx = (fr < 8) ? 0x4444 : 0xEEEE;
    const int ao = a_off(lane);
    for (int s = 0; s < NSEG; ++s) {
        if (wid == 0) TS(s, 0);
        gather_mem(a, rs, lds, s, wid, lane, tid, dead);
        if (wid == 0) TS(s, 1);
        RAW_BARRIER();
        if (wid == 0) TS(s, 2);
        {
            f32x4 c0 = {0.f, 0.f, 0.f, 0.f};
#pragma unroll
            for (int c = 0; c < 10; ++c) {
                const u32x4 av = cat(trread(lds + L_MEMI + c * 1024 + ao), trread(lds + L_MEMI + c * 1024 + (ao ^ 128)));
                c0 = smfmac(av, Wv[c], c0, idx);
            }
            if (s + 1 < NSEG - 1) dma(s + 1);
            const u32 d = pack2(fold32(c0[0], c0[2]), fold32(c0[1], c0[3]));
            *(LDSP u32*)(lds + L_QKI + memi_unit(wid * 16 + fr) * 16 + (2 * (g & 1) + (g >> 1)) * 4) = d;
        }
        RAW_BARRIER();
        if (wid == 0) TS(s, 3);
        {
            f32x4 u[5];
#pragma unroll
            for (int t = 0; t < 5; ++t) u[t] = (f32x4){0.f, 0.f, 0.f, 0.f};
#pragma unroll
            for (int c = 0; c < 2; ++c) {
                const u32x4 av = cat(trread(lds + L_QKI + c * 1024 + ao), trread(lds + L_QKI + c * 1024 + (ao ^ 128)));
#pragma unroll
                for (int t = 0; t < 5; ++t) u[t] = smfmac(av, Wo[t][c], u[t], idx);
            }
            u32 x0 = pack2(fold32(u[0][0], u[1][0]), fold32(u[0][1], u[1][1])), x1 = pack2(fold32(u[0][2], u[1][2]), fold32(u[0][3], u[1][3]));
            u32 y0 = pack2(fold32(u[2][0], u[3][0]), fold32(u[2][1], u[3][1])), y1 = pack2(fold32(u[2][2], u[3][2]), fold32(u[2][3], u[3][3]));
            swap16(x0, y0); swap16(x1, y1);
            const u32 ub = OFF_U + (u32)(s * 32 + h) * 10240u + wid * 1280;
            bst128<16>(rws, (u32x4){nosent(x0), nosent(x1), nosent(y0), nosent(y1)}, ub + (((g & 1) * 2 + (g >> 1)) * 16 + fr) * 16);
            u32 e0 = pack2(fold32(u[4][0], u[4][2]), fold32(u[4][1], u[4][3])), e1 = e0;
            swap32(e0, e1);
            u32 f0 = e0, f1 = e1;
            swap16(e0, f0); swap16(e1, f1);
            if (g == 0) bst128<16>(rws, (u32x4){nosent(e0), nosent(e1), nosent(f0), nosent(f1)}, ub + (64 + fr) * 16);
        }
        if (wid == 0) TS(s, 4);
        if (s + 1 < NSEG) {
            if (wid < ntile) {
                u32x4 yb[NPRE], yu[4];
                lptr rg = lds + L_RING + (s & 1) * 49152 + wid * 16384 + lane * 16;
#pragma unroll
                for (int hh = 0; hh < NPRE; ++hh) yb[hh] = lds128(rg + hh * 1024);
                const u32 vo = (u32)g * 10240u + ((tile0 + wid) * 16 + fr) * 16;
                const u32 so = OFF_U + (u32)(s * 32 + grp * 16) * 10240u;
                int spins = 0;
                for (;;) {
                    ld4v(rs, vo, vo + 4 * 10240, vo + 8 * 10240, vo + 12 * 10240, so, yu);
                    const u32 mx = umax(umax(max4(yu[0]), max4(yu[1])), umax(max4(yu[2]), max4(yu[3])));
                    if (__all(mx != SENT) || dead) break;
                    if (++spins > SPIN_CAP) dead = true;
                }
                if (wid == 0) TS(s, 6);
                RAW_BARRIER();
                if (wid == 0) TS(s, 7);
                f32x4 c0 = {0.f, 0.f, 0.f, 0.f};
                lptr px = lds + L_PI + (lane >> 5) * 640 + (lane & 31) * 16;
#pragma unroll
                for (int c = 0; c < NPRE / 2; ++c) c0 = smfmac(lds128(px + c * 1280), cat(yb[2 * c], yb[2 * c + 1]), c0, idx);
#pragma unroll
                for (int c = 0; c < 2; ++c) {
                    lptr pm = lds + L_PI + 512 + c * 5120 + g * 1280 + (fr & 7) * 16 + (fr >> 3) * 8;
                    c0 = smfmac(cat(*(LDSP u32x2*)pm, *(LDSP u32x2*)(pm + 640)), cat(yu[2 * c], yu[2 * c + 1]), c0, idx);
                }
#pragma unroll
                for (int c = NPRE / 2; c < 8; ++c)
                    c0 = smfmac(lds128(px + c * 1280), cat(lds128(rg + 2 * c * 1024), lds128(rg + 2 * c * 1024 + 1024)), c0, idx);
                u32 e0 = pack2(fold32(c0[0], c0[2]), fold32(c0[1], c0[3])), e1 = e0;
                swap32(e0, e1);
                u32 f0 = e0, f1 = e1;
                swap16(e0, f0); swap16(e1, f1);
                if (g == 0) bst128<16>(rws, (u32x4){nosent(e0), nosent(e1), nosent(f0), nosent(f1)},
                                       OFF_MX + (u32)((s + 1) * 2 + grp) * 10240u + ((tile0 + wid) * 16 + fr) * 16);
            } else {
                if (wid >= 3 && wid < 7) {
                    const int c0 = wid < 5 ? (wid - 3) * 3 : 6 + (wid - 5) * 2, c2 = wid < 5 ? c0 + 2 : c0 + 1;
                    const u32 so = OFF_P + (u32)(s * 2 + grp) * 10240u;
                    u32x4 t[3];
                    int spins = 0;
                    for (;;) {
                        ld3v(rs, c0 * 1024 + lane * 16, c2 * 1024 + lane * 16, so, t);
                        const u32 mx = umax(umax(max4(t[0]), max4(t[1])), max4(t[2]));
                        if (__all(mx != SENT) || dead) break;
                        if (++spins > SPIN_CAP) dead = true;
                    }
                    *(LDSP u32x4*)(lds + L_PI + c0 * 1024 + lane * 16) = t[0];
                    *(LDSP u32x4*)(lds + L_PI + c0 * 1024 + 1024 + lane * 16) = t[1];
                    *(LDSP u32x4*)(lds + L_PI + c2 * 1024 + lane * 16) = t[2];
                    if (wid == 3) TS(s, 8);
                } else {
                    if (wid == 7 && ((s + 2) & 3) == 0 && s + 2 < NSEG - 1) wait_flag(flags, F_VB + ((s + 2) >> 2) * 2 + grp, 16, dead);
                    WAIT_V(0);
                }
                RAW_BARRIER();
                if (s == NSEG - 2 && tid == 448) flag_add(flags, F_X + s);
            }
        }
    }
    WAIT_V(0);
    RAW_BARRIER();
    if (tid == 0) flag_add(flags, F_X + NSEG - 1);
}

__global__ void __launch_bounds__(512, 2) main_kernel(Args a) {
    __shared__ __attribute__((aligned(1024))) char smem[163840];
    lptr lds = (lptr)smem;
    const int tid = threadIdx.x, lane = tid & 63, wid = __builtin_amdgcn_readfirstlane(tid >> 6);
    if (blockIdx.x >= 64) worker(a, lds, (LDSP int*)(lds + 163824), wid, lane, tid);
    else if (blockIdx.x < 32) chain_s(a, lds, blockIdx.x, wid, lane, tid);
    else chain_v(a, lds, blockIdx.x - 32, wid, lane, tid);
}

extern "C" void kernel_launch(void* const* d_in, const int* in_sizes, int n_in, void* d_out, int out_size, void* d_ws, size_t ws_size,
                              hipStream_t stream) {
    Args a;
    a.x = (const char*)d_in[0]; a.mem0 = (const char*)d_in[1]; a.Wq = (const char*)d_in[2]; a.Wk = (const char*)d_in[3];
    a.Wv = (const char*)d_in[4]; a.Wo = (const char*)d_in[5]; a.w1 = (const char*)d_in[8]; a.b1 = (const char*)d_in[9];
    a.w2 = (const char*)d_in[10]; a.b2 = (const char*)d_in[11];
    a.out = (char*)d_out; a.ws = (char*)d_ws;
    prologue_kernel<<<512, 256, 0, stream>>>((const u32*)d_in[0], (char*)d_ws);
    main_kernel<<<256, 512, 0, stream>>>(a);
}
```

```cpp
#include <hip/hip_runtime.h>
#include <hip/hip_bf16.h>
#include <stdint.h>
#pragma clang diagnostic ignored "-Winline-asm"

typedef unsigned u32;
typedef u32 u32x2 __attribute__((ext_vector_type(2)));
typedef u32 u32x4 __attribute__((ext_vector_type(4)));
typedef u32 u32x8 __attribute__((ext_vector_type(8)));
typedef float f32x4 __attribute__((ext_vector_type(4)));
typedef float f32x2 __attribute__((ext_vector_type(2)));
typedef __bf16 bf16x2 __attribute__((ext_vector_type(2)));
typedef __bf16 bf16x8 __attribute__((ext_vector_type(8)));
typedef __bf16 bf16x16 __attribute__((ext_vector_type(16)));
typedef short s16x4 __attribute__((ext_vector_type(4)));
#define LDSP __attribute__((address_space(3)))
typedef LDSP char* lptr;
#define DEV __device__ __forceinline__

constexpr int SEQ = 2048, DM = 640, NH = 32, DH = 128, NSEG = 64, FF = 1728;
constexpr u32 SENT = 0xFFFFFFFFu;
constexpr u32 WQ_BYTES = 640u * 4096u * 2u, W1_BYTES = 640u * 1728u * 2u;
constexpr int SPIN_CAP = 400000;

constexpr u32 MB = 1u << 20;
constexpr u32 OFF_FLAGS = 0;
constexpr u32 FLAG_BYTES = 128 * 1024;
constexpr u32 OFF_X = 2 * MB, XP_PX = 12288, XP_PM = 28672;
constexpr u32 OFF_U = 6 * MB;
constexpr u32 SENT_END = OFF_U + 64u * 32u * 10240u;
constexpr u32 OFF_XN = 32 * MB;
constexpr u32 OFF_G = 36 * MB;
constexpr u32 OFF_H = 40 * MB;
constexpr u32 OFF_PX = 48 * MB;
constexpr u32 OFF_PXM = 54 * MB;
constexpr u32 OFF_Q = 56 * MB;
constexpr u32 OFF_K = 76 * MB;
constexpr u32 OFF_B = 96 * MB;
constexpr u32 WS_USED = 176 * MB;
constexpr int F_QX = 0, F_QG = 8, F_PAR = 9, F_VB = 16  , F_H = 48  , F_G = 64  , F_X = 96  , F_KQ = 256  ;
constexpr u32 FX_FULL = 320;

#define TC_DECL do {} while (0)
#define TC(k) do {} while (0)
#define TCV(k, v) do {} while (0)
#define TC_FLUSH(s) do {} while (0)
#define TC_DUMP() do {} while (0)
#define TS(slot, k) do {} while (0)
#define TSV(slot, k, v) do {} while (0)
#define WAIT_V(n) do { asm volatile("" ::: "memory"); __builtin_amdgcn_s_waitcnt(0xF70 | ((n) & 15) | (((n) >> 4) << 14)); asm volatile("" ::: "memory"); } while (0)
#define WAIT_L0() do { asm volatile("" ::: "memory"); __builtin_amdgcn_s_waitcnt(0xC07F); asm volatile("" ::: "memory"); } while (0)
#define RAW_BARRIER() do { WAIT_L0(); __builtin_amdgcn_s_barrier(); asm volatile("" ::: "memory"); } while (0)

DEV u32 pack2(float a, float b) { bf16x2 v = {(__bf16)a, (__bf16)b}; return __builtin_bit_cast(u32, v); }
DEV float bfr(float a) { return (float)(__bf16)a; }
DEV float blo(u32 d) { return __builtin_bit_cast(float, d << 16); }
DEV float bhi(u32 d) { return __builtin_bit_cast(float, d & 0xffff0000u); }
DEV u32 fbits(float f) { return __builtin_bit_cast(u32, f); }
DEV float bitsf(u32 u) { return __builtin_bit_cast(float, u); }
DEV u32 nosent(u32 d) { return d < 0xFFFFFFFEu ? d : 0xFFFFFFFEu; }
DEV void swap32(u32& a, u32& b) { auto r = __builtin_amdgcn_permlane32_swap(a, b, false, false); a = r[0]; b = r[1]; }
DEV void swap16(u32& a, u32& b) { auto r = __builtin_amdgcn_permlane16_swap(a, b, false, false); a = r[0]; b = r[1]; }
DEV float fold32(float a, float b) { u32 x = fbits(a), y = fbits(b); swap32(x, y); return bitsf(x) + bitsf(y); }
DEV float xmax(float v) {
    u32 a = fbits(v), b = a; swap32(a, b); v = fmaxf(bitsf(a), bitsf(b));
    a = fbits(v); b = a; swap16(a, b); return fmaxf(bitsf(a), bitsf(b));
}
DEV float xsum(float v) {
    u32 a = fbits(v), b = a; swap32(a, b); v = bitsf(a) + bitsf(b);
    a = fbits(v); b = a; swap16(a, b); return bitsf(a) + bitsf(b);
}
DEV u32x2 trread(lptr p) { s16x4 v = __builtin_amdgcn_ds_read_tr16_b64_v4i16((LDSP s16x4*)p); return __builtin_bit_cast(u32x2, v); }
DEV u32x4 lds128(lptr p) { return *(LDSP u32x4*)p; }
DEV u32x4 cat(u32x2 a, u32x2 b) { return (u32x4){a.x, a.y, b.x, b.y}; }
DEV u32x8 cat(u32x4 a, u32x4 b) { return (u32x8){a.x, a.y, a.z, a.w, b.x, b.y, b.z, b.w}; }
DEV f32x4 mfma(u32x4 a, u32x4 b, f32x4 c) {
    return __builtin_amdgcn_mfma_f32_16x16x32_bf16(__builtin_bit_cast(bf16x8, a), __builtin_bit_cast(bf16x8, b), c, 0, 0, 0);
}
DEV f32x4 smfmac(u32x4 a, u32x8 b, f32x4 c, int idx) {
    return __builtin_amdgcn_smfmac_f32_16x16x64_bf16(__builtin_bit_cast(bf16x8, a), __builtin_bit_cast(bf16x16, b), c, idx, 0, 0);
}
template <int SC1> DEV void bdma(u32x4 rs, u32 vo, u32 so, u32 lo) {
    if (SC1) asm volatile("s_mov_b32 m0, %3\n\ts_nop 3\n\tbuffer_load_dwordx4 %0, %1, %2 offen sc1 lds" ::"v"(vo), "s"(rs), "s"(so), "s"(lo) : "memory", "m0");
    else asm volatile("s_mov_b32 m0, %3\n\ts_nop 3\n\tbuffer_load_dwordx4 %0, %1, %2 offen lds" ::"v"(vo), "s"(rs), "s"(so), "s"(lo) : "memory", "m0");
}
DEV u32x4 mkrsrc(const void* p, u32 bytes) {
    unsigned long long a = (unsigned long long)p;
    u32x4 r = {(u32)a, (u32)(a >> 32) & 0xffffu, bytes, 0x00020000u};
    r.x = __builtin_amdgcn_readfirstlane(r.x); r.y = __builtin_amdgcn_readfirstlane(r.y);
    r.z = __builtin_amdgcn_readfirstlane(r.z); r.w = __builtin_amdgcn_readfirstlane(r.w);
    return r;
}
DEV char* ws_base(char* ws) { return ws + ((0u - (u32)(unsigned long long)ws) & (2 * MB - 1)); }
DEV u32 blk(u32 o) { return (o >> 13) * 16384u + (o & 8191u); }
DEV u32 xstep(int s, u32 par) { return OFF_X + (u32)s * 65536u + ((par >> (s >> 5)) & 1u) * 8192u; }
DEV int opq(int v) { asm volatile("" : "+v"(v)); return v; }
DEV u32 cyc() { return (u32)__builtin_amdgcn_s_memtime(); }
DEV u32 now4(u32 base) { return ((u32)__builtin_amdgcn_s_memrealtime() - base) * 4u; }
DEV void wait_slot(u32 t, u32 base) {
    int rem;
    for (;;) {
        rem = (int)(t - now4(base));
        if (rem <= 32 || rem > 8000) break;
        if (rem > 100) __builtin_amdgcn_s_sleep(1);
    }
#pragma unroll
    for (int i = 0; i < 5; ++i)
        if (rem >= 19 + 3 * i && rem <= 32) asm volatile("s_nop 11");
}
DEV u32 next_slot(u32 t, u32 T, u32 gap, u32 base) {
    const u32 lim = now4(base) + gap;
    int n = 0;
    do t += T; while ((int)(t - lim) < 0 && ++n < 4096);
    return t;
}
DEV u32 max4(u32x4 v) { u32 a = v.x > v.y ? v.x : v.y, b = v.z > v.w ? v.z : v.w; return a > b ? a : b; }
DEV u32 umax(u32 a, u32 b) { return a > b ? a : b; }

typedef __amdgpu_buffer_rsrc_t rsrc_t;
template <int AUX> DEV void bst128(rsrc_t r, u32x4 v, u32 off) { __builtin_amdgcn_raw_buffer_store_b128(v, r, off, 0, AUX); }
template <int AUX> DEV void bst64(rsrc_t r, u32x2 v, u32 off) { __builtin_amdgcn_raw_buffer_store_b64(v, r, off, 0, AUX); }
template <int AUX> DEV void bst32(rsrc_t r, u32 v, u32 off) { __builtin_amdgcn_raw_buffer_store_b32(v, r, off, 0, AUX); }
DEV u32x4 bld128s(rsrc_t r, u32 off) { return __builtin_amdgcn_raw_buffer_load_b128(r, off, 0, 16); }

DEV void wait_flag(u32* flags, int idx, u32 target, bool& dead) {
    if (dead) return;
    int spins = 0;
    for (;;) {
        u32 v = __hip_atomic_load(flags + idx * 32, __ATOMIC_RELAXED, __HIP_MEMORY_SCOPE_AGENT);
        if (__builtin_amdgcn_readfirstlane(v) >= target) break;
        if (++spins > SPIN_CAP) { dead = true; break; }
        __builtin_amdgcn_s_sleep(8);
    }
    asm volatile("" ::: "memory");
}
DEV void flag_add(u32* flags, int idx) { __hip_atomic_fetch_add(flags + idx * 32, 1u, __ATOMIC_RELAXED, __HIP_MEMORY_SCOPE_AGENT); }

__global__ void __launch_bounds__(256) prologue_kernel(const u32* __restrict__ x, char* __restrict__ ws0) {
    char* ws = ws_base(ws0);
    const int tid = threadIdx.x, lane = tid & 63, wid = tid >> 6;
    const int row = blockIdx.x * 4 + wid;
    u32 d[5];
#pragma unroll
    for (int i = 0; i < 5; ++i) d[i] = x[row * 320 + i * 64 + lane];
    {
        const u32x4 s = {SENT, SENT, SENT, SENT};
        constexpr u32 n16 = (SENT_END - OFF_X) / 16;
        u32x4* p = (u32x4*)(ws + OFF_X);
        for (u32 i = blockIdx.x * 256 + tid; i < n16; i += 512 * 256) p[i] = s;
        const u32x4 z = {0, 0, 0, 0};
        u32 j = blockIdx.x * 256 + tid;
        if (j < FLAG_BYTES / 16) ((u32x4*)(ws + OFF_FLAGS))[j] = z;
    }
    float v[10], s = 0.f;
#pragma unroll
    for (int i = 0; i < 5; ++i) { v[2 * i] = blo(d[i]); v[2 * i + 1] = bhi(d[i]); s += v[2 * i] + v[2 * i + 1]; }
#pragma unroll
    for (int o = 32; o; o >>= 1) s += __shfl_xor(s, o);
    const float mean = s * (1.0f / DM);
    float q = 0.f;
#pragma unroll
    for (int i = 0; i < 10; ++i) { float t = v[i] - mean; q += t * t; }
#pragma unroll
    for (int o = 32; o; o >>= 1) q += __shfl_xor(q, o);
    const float mb = bfr(mean), vb = bfr(q * (1.0f / DM));
    const float den = bfr(sqrtf(bfr(vb + 1e-5f)));
    u32* xn = (u32*)(ws + OFF_XN);
#pragma unroll
    for (int i = 0; i < 5; ++i) xn[row * 320 + i * 64 + lane] = pack2(bfr(v[2 * i] - mb) / den, bfr(v[2 * i + 1] - mb) / den);
}

template <int NT>
DEV void gemm_core(f32x4 (&acc)[4][NT], u32x4 rX, u32 xoff, int ldx, u32x4 rW0, u32x4 rW1, u32 woff0, u32 woff1, int ldw, int chmax1, int nkt,
                   lptr lds, int wid, int lane) {
    constexpr int NP = NT / 2, STAGE = 16384 * (1 + NP), G = 2 + 2 * NP;
    const int sb = lane * 16, swz = sb ^ (((sb >> 9) & 1) << 5);
    const u32 xv = (u32)(16 * wid + (swz >> 6)) * ldx + (swz & 63);
    u32 wv0[2], wv1[2];
#pragma unroll
    for (int i = 0; i < 2; ++i) {
        const int row = 8 * wid + 4 * i + (lane >> 4);
        const int ch = (lane & 15) ^ (((row & 3) << 2) | ((row >> 2) & 3));
        wv0[i] = (u32)row * ldw + ch * 16;
        wv1[i] = (u32)row * ldw + (ch < chmax1 ? ch : chmax1) * 16;
    }
    const u32 ldsb = (u32)(size_t)lds;
    auto issue = [&](int kt, int buf) {
        const u32 b = ldsb + buf * STAGE;
        bdma<1>(rX, xv, xoff + kt * 128, b + wid * 2048);
        bdma<1>(rX, xv, xoff + kt * 128 + 64, b + wid * 2048 + 1024);
        const u32 wk = (u32)kt * 64u * ldw;
#pragma unroll
        for (int i = 0; i < 2; ++i) {
            bdma<0>(rW0, wv0[i], woff0 + wk, b + 16384 + (8 * wid + 4 * i) * 256);
            if (NP == 2) bdma<0>(rW1, wv1[i], woff1 + wk, b + 32768 + (8 * wid + 4 * i) * 256);
        }
    };
    const int wr = wid >> 2, wc = wid & 3, g = lane >> 4, q = (lane >> 2) & 3, p = lane & 3;
    const int ob = (lane & 15) * 64 + g * 16;
    const int xo = wr * 8192 + (ob ^ (((ob >> 9) & 1) << 5));
    int wo[NT][2];
#pragma unroll
    for (int n = 0; n < NT; ++n)
#pragma unroll
        for (int t = 0; t < 2; ++t) {
            const int panel = (NT == 4) ? (wc >> 1) : 0;
            const int c = (NT == 4) ? ((wc & 1) * 4 + n) : (wc * 2 + n);
            wo[n][t] = 16384 * (1 + panel) + 256 * (8 * g + 4 * t + q) + 16 * ((2 * c + (p >> 1)) ^ ((q << 2) | ((2 * g + t) & 3))) + 8 * (p & 1);
        }
#pragma unroll
    for (int m = 0; m < 4; ++m)
#pragma unroll
        for (int n = 0; n < NT; ++n) acc[m][n] = (f32x4){0.f, 0.f, 0.f, 0.f};
    issue(0, 0);
    issue(1, 1);
    int buf = 0;
    for (int kt = 0; kt < nkt; ++kt) {
        if (kt + 1 < nkt) WAIT_V(G); else WAIT_V(0);
        RAW_BARRIER();
        if (kt + 2 < nkt) issue(kt + 2, buf >= 1 ? buf - 1 : 2);
        lptr b = lds + buf * STAGE;
#pragma unroll
        for (int ks = 0; ks < 2; ++ks) {
            u32x4 xf[4], wf[NT];
#pragma unroll
            for (int m = 0; m < 4; ++m) xf[m] = lds128(b + xo + (m * 2 + ks) * 1024);
#pragma unroll
            for (int n = 0; n < NT; ++n) wf[n] = cat(trread(b + wo[n][0] + ks * 8192), trread(b + wo[n][1] + ks * 8192));
#pragma unroll
            for (int m = 0; m < 4; ++m)
#pragma unroll
                for (int n = 0; n < NT; ++n) acc[m][n] = mfma(wf[n], xf[m], acc[m][n]);
        }
        buf = (buf == 2) ? 0 : buf + 1;
    }
}
DEV u32x4 pair8(u32 a0, u32 a1, u32 b0, u32 b1) { swap16(a0, b0); swap16(a1, b1); return (u32x4){a0, a1, b0, b1}; }

struct Args {
    const char *x, *mem0, *Wq, *Wk, *Wv, *Wo, *w1, *b1, *w2, *b2;
    char *out, *ws;
    int T, fs, fv, fp, fu, gs, gv, gc;
};

DEV void worker(const Args& a, lptr lds, LDSP int* bc, int wid, int lane, int tid) {
    char* ws = a.ws;
    u32* flags = (u32*)(ws + OFF_FLAGS);
    const rsrc_t rws = __builtin_amdgcn_make_buffer_rsrc(ws, 0, WS_USED, 0x00020000);
    const u32x4 rs = mkrsrc(ws, WS_USED);
    const u32 ldsb = (u32)(size_t)lds;
    const int xq = blockIdx.x & 3;
    const int wr = wid >> 2, wc = wid & 3, g = lane >> 4, fr = lane & 15;
    const int c8 = 8 * ((g >> 1) + 2 * (g & 1));
    int qi = 0;
    bool dead = false;
    int nit = 0;
    for (;;) {
        if (wid == 0 && nit < 80) TS(nit, 0);
        if (tid == 0) {
            int type = -1, p0 = 0, p1 = 0;
            while (qi < 4) {
                const int qq = (xq + qi) & 3;
                const int it = (int)__hip_atomic_fetch_add(flags + (F_QX + qq) * 32, 1u, __ATOMIC_RELAXED, __HIP_MEMORY_SCOPE_AGENT);
                if (it < 256) { type = (it & 1) ^ 1; p0 = it >> 4; p1 = qq * 8 + ((it & 15) >> 1); break; }
                ++qi;
            }
            if (type < 0) {
                const int it = (int)__hip_atomic_fetch_add(flags + F_QG * 32, 1u, __ATOMIC_RELAXED, __HIP_MEMORY_SCOPE_AGENT);
                if (it < 112) { type = 2; p0 = it / 7; p1 = it % 7; }
                else if (it < 192) { type = 3; p0 = (it - 112) / 5; p1 = (it - 112) % 5; }
                else if (it < 192 + 640) { type = 4; p0 = (it - 192) / 10; p1 = (it - 192) % 10; }
            }
            bc[0] = type; bc[1] = p0; bc[2] = p1;
        }
        RAW_BARRIER();
        const int type = __builtin_amdgcn_readfirstlane(bc[0]), p0 = __builtin_amdgcn_readfirstlane(bc[1]), p1 = __builtin_amdgcn_readfirstlane(bc[2]);
        RAW_BARRIER();
        if (type < 0) break;
        if (wid == 0 && nit < 80) { TS(nit, 1); TSV(nit, 2, type * 65536 + p0 * 256 + p1 + 1); }
        ++nit;
        if (type == 0 || type == 2) {
            f32x4 acc[4][4];
            {
                const bool qk = type == 0;
                const u32x4 r0 = mkrsrc(qk ? a.Wq : a.w1, qk ? WQ_BYTES : W1_BYTES), r1 = mkrsrc(qk ? a.Wk : a.w1, qk ? WQ_BYTES : W1_BYTES);
                const u32 o0 = qk ? p1 * 256 : p1 * 512, o1 = qk ? o0 : o0 + 256;
                gemm_core<4>(acc, rs, OFF_XN + (u32)p0 * 128u * 1280u, 1280, r0, r1, o0, o1, qk ? 8192 : FF * 2, (!qk && p1 == 6) ? 7 : 15, 10, lds, wid, lane);
            }
            if (type == 0) {
                const u32 base = ((wc >> 1) ? OFF_K : OFF_Q) + (u32)(p0 * 128 + wr * 64 + fr) * 8192u + p1 * 256 + ((wc & 1) * 64 + c8) * 2;
#pragma unroll
                for (int m = 0; m < 4; ++m)
#pragma unroll
                    for (int n = 0; n < 4; n += 2) {
                        u32x4 v = pair8(pack2(acc[m][n][0], acc[m][n][1]), pack2(acc[m][n][2], acc[m][n][3]),
                                        pack2(acc[m][n + 1][0], acc[m][n + 1][1]), pack2(acc[m][n + 1][2], acc[m][n + 1][3]));
                        bst128<16>(rws, v, base + m * 16 * 8192 + n * 32);
                    }
            } else {
                const int col0 = p1 * 256 + wc * 64;
                u32x2 bb[4];
#pragma unroll
                for (int n = 0; n < 4; ++n) { int c = col0 + n * 16 + 4 * g; c = c < FF - 4 ? c : FF - 4; bb[n] = *(const u32x2*)(a.b1 + c * 2); }
                const u32 base = OFF_H + (u32)(p0 * 128 + wr * 64 + fr) * (FF * 2) + (col0 + c8) * 2;
#pragma unroll
                for (int m = 0; m < 4; ++m) {
                    u32 h[4][2];
#pragma unroll
                    for (int n = 0; n < 4; ++n) {
                        float t0 = fmaxf(bfr(bfr(acc[m][n][0]) + blo(bb[n].x)), 0.f), t1 = fmaxf(bfr(bfr(acc[m][n][1]) + bhi(bb[n].x)), 0.f);
                        float t2 = fmaxf(bfr(bfr(acc[m][n][2]) + blo(bb[n].y)), 0.f), t3 = fmaxf(bfr(bfr(acc[m][n][3]) + bhi(bb[n].y)), 0.f);
                        h[n][0] = pack2(t0 * t0, t1 * t1); h[n][1] = pack2(t2 * t2, t3 * t3);
                    }
#pragma unroll
                    for (int n = 0; n < 4; n += 2) {
                        u32x4 v = pair8(h[n][0], h[n][1], h[n + 1][0], h[n + 1][1]);
                        if (col0 < FF) bst128<16>(rws, v, base + m * 16 * (FF * 2) + n * 32);
                    }
                }
            }
            WAIT_V(0);
            RAW_BARRIER();
            if (tid == 0) flag_add(flags, type == 0 ? F_KQ + p0 * 32 + p1 : F_H + p0);
        } else if (type == 1 || type == 3) {
            f32x4 acc[4][2];
            u32 wop[4];
            const u32x4 rWo = mkrsrc(a.Wo, WQ_BYTES);
            const u32 wob = (u32)p1 * 128u * 1280u;
            if (type == 1) {
#pragma unroll
                for (int i = 0; i < 4; ++i) {
                    const int row = 16 * wid + 4 * i + g;
                    const int ch = fr ^ (((row & 3) << 2) | ((row >> 2) & 3));
                    wop[i] = (u32)row * 1280u + ch * 16;
                    bdma<0>(rWo, wop[i], wob, ldsb + 3 * 32768 + (16 * wid + 4 * i) * 256);
                }
            } else {
                wait_flag(flags, F_H + p0, 7, dead);
            }
            {
                const bool v = type == 1;
                const u32x4 r0 = mkrsrc(v ? a.Wv : a.w2, v ? WQ_BYTES : W1_BYTES);
                gemm_core<2>(acc, rs, v ? OFF_XN + (u32)p0 * 128u * 1280u : OFF_H + (u32)p0 * 128u * (FF * 2), v ? 1280 : FF * 2, r0, r0, p1 * 256, 0,
                             v ? 8192 : 1280, 15, v ? 10 : 27, lds, wid, lane);
            }
            if (type == 1) {
                {
                    const int ob = fr * 64 + c8 * 2;
                    lptr vi = lds + 4 * 32768 + wc * 1024 + (ob ^ (((ob >> 9) & 1) << 5));
#pragma unroll
                    for (int m = 0; m < 4; ++m)
                        *(LDSP u32x4*)(vi + (wr * 4 + m) * 4096) = pair8(pack2(acc[m][0][0], acc[m][0][1]), pack2(acc[m][0][2], acc[m][0][3]),
                                                                          pack2(acc[m][1][0], acc[m][1][1]), pack2(acc[m][1][2], acc[m][1][3]));
                }
                RAW_BARRIER();
#pragma unroll
                for (int pn = 1; pn < 4; ++pn)
#pragma unroll
                    for (int i = 0; i < 4; ++i) bdma<0>(rWo, wop[i], wob + pn * 256, ldsb + (pn - 1) * 32768 + (16 * wid + 4 * i) * 256);
                u32x4 vf[4][4];
                {
                    const int ob = fr * 64 + g * 16;
                    lptr vi = lds + 4 * 32768 + wr * 16384 + (ob ^ (((ob >> 9) & 1) << 5));
#pragma unroll
                    for (int m = 0; m < 4; ++m)
#pragma unroll
                        for (int ks = 0; ks < 4; ++ks) vf[m][ks] = lds128(vi + m * 4096 + ks * 1024);
                }
                RAW_BARRIER();
#pragma unroll
                for (int i = 0; i < 4; ++i) bdma<0>(rWo, wop[i], wob + 4 * 256, ldsb + 4 * 32768 + (16 * wid + 4 * i) * 256);
                const int q = (lane >> 2) & 3, p = lane & 3;
                int wo[2][2];
#pragma unroll
                for (int n = 0; n < 2; ++n)
#pragma unroll
                    for (int t = 0; t < 2; ++t)
                        wo[n][t] = 256 * (8 * g + 4 * t + q) + 16 * ((2 * (wc * 2 + n) + (p >> 1)) ^ ((q << 2) | ((2 * g + t) & 3))) + 8 * (p & 1);
                for (int pn = 0; pn < 5; ++pn) {
                    if (pn) { WAIT_V(16); RAW_BARRIER(); }
                    lptr b = lds + (pn == 0 ? 3 : (pn == 4 ? 4 : pn - 1)) * 32768;
                    f32x4 c2[4][2];
#pragma unroll
                    for (int m = 0; m < 4; ++m) { c2[m][0] = (f32x4){0.f, 0.f, 0.f, 0.f}; c2[m][1] = c2[m][0]; }
#pragma unroll
                    for (int ks = 0; ks < 4; ++ks) {
                        u32x4 wf[2];
#pragma unroll
                        for (int n = 0; n < 2; ++n) wf[n] = cat(trread(b + wo[n][0] + ks * 8192), trread(b + wo[n][1] + ks * 8192));
#pragma unroll
                        for (int m = 0; m < 4; ++m)
#pragma unroll
                            for (int n = 0; n < 2; ++n) c2[m][n] = mfma(vf[m][ks], wf[n], c2[m][n]);
                    }
#pragma unroll
                    for (int sg = 0; sg < 2; ++sg)
#pragma unroll
                        for (int n = 0; n < 2; ++n) {
                            u32x4 v = {pack2(c2[2 * sg][n][0], c2[2 * sg][n][1]), pack2(c2[2 * sg][n][2], c2[2 * sg][n][3]),
                                       pack2(c2[2 * sg + 1][n][0], c2[2 * sg + 1][n][1]), pack2(c2[2 * sg + 1][n][2], c2[2 * sg + 1][n][3])};
                            const u32 seg = p0 * 4 + wr * 2 + sg;
                            bst128<16>(rws, v, OFF_B + ((seg * 32 + p1) * 40 + pn * 8 + wc * 2 + n) * 1024u + lane * 16);
                        }
                }
            } else {
                const int col0 = p1 * 128 + wc * 32;
                u32x2 bb[2];
#pragma unroll
                for (int n = 0; n < 2; ++n) bb[n] = *(const u32x2*)(a.b2 + (col0 + n * 16 + 4 * g) * 2);
                const u32 base = OFF_G + (u32)(p0 * 128 + wr * 64 + fr) * 1280u + (col0 + c8) * 2;
#pragma unroll
                for (int m = 0; m < 4; ++m) {
                    u32 h[2][2];
#pragma unroll
                    for (int n = 0; n < 2; ++n) {
                        float t[4] = {bfr(bfr(acc[m][n][0]) + blo(bb[n].x)), bfr(bfr(acc[m][n][1]) + bhi(bb[n].x)),
                                      bfr(bfr(acc[m][n][2]) + blo(bb[n].y)), bfr(bfr(acc[m][n][3]) + bhi(bb[n].y))};
#pragma unroll
                        for (int r = 0; r < 4; ++r) t[r] = t[r] / (1.0f + __expf(-t[r]));
                        h[n][0] = pack2(t[0], t[1]); h[n][1] = pack2(t[2], t[3]);
                    }
                    bst128<16>(rws, pair8(h[0][0], h[0][1], h[1][0], h[1][1]), base + m * 16 * 1280);
                }
            }
            WAIT_V(0);
            RAW_BARRIER();
            if (tid == 0) flag_add(flags, type == 1 ? F_VB + p0 * 2 + (p1 >> 4) : F_G + p0);
        } else {
            const int seg = p0, cg = p1;
            wait_flag(flags, F_VB + (seg >> 2) * 2 + (wid >> 2), 16, dead);
            u32x4 yb[4][4], yu[4], px[4][2], pm[2];
#pragma unroll
            for (int hh = 0; hh < 4; ++hh)
#pragma unroll
                for (int n = 0; n < 4; ++n) yb[hh][n] = bld128s(rws, OFF_B + ((seg * 32 + wid * 4 + hh) * 40 + cg * 4 + n) * 1024u + lane * 16);
            wait_flag(flags, F_G + (seg >> 2), 5, dead);
            const u32 eo = (u32)(seg * 32 + (wid >> 2) * 16 + fr) * 1280u + (cg * 64 + (wid & 3) * 16 + 4 * g) * 2;
            const u32x2 xv = *(const u32x2*)(a.x + eo);
            const u32x2 gv = __builtin_amdgcn_raw_buffer_load_b64(rws, OFF_G + eo, 0, 16);
            wait_flag(flags, F_X + seg, FX_FULL, dead);
#pragma unroll
            for (int hh = 0; hh < 4; ++hh)
#pragma unroll
                for (int qt = 0; qt < 2; ++qt) px[hh][qt] = bld128s(rws, OFF_PX + (seg * 32 + wid * 4 + hh) * 2048u + qt * 1024 + lane * 16);
#pragma unroll
            for (int qt = 0; qt < 2; ++qt) pm[qt] = bld128s(rws, OFF_PXM + (seg * 8 + wid) * 2048u + qt * 1024 + lane * 16);
#pragma unroll
            for (int n = 0; n < 4; ++n) yu[n] = bld128s(rws, OFF_U + (seg * 32 + wid * 4 + g) * 10240u + (cg * 64 + n * 16 + fr) * 16);
            f32x4 c[2][4];
#pragma unroll
            for (int qt = 0; qt < 2; ++qt)
#pragma unroll
                for (int n = 0; n < 4; ++n) {
                    f32x4 t = {0.f, 0.f, 0.f, 0.f};
#pragma unroll
                    for (int hh = 0; hh < 4; ++hh) t = mfma(yb[hh][n], px[hh][qt], t);
                    c[qt][n] = mfma(yu[n], pm[qt], t);
                }
#pragma unroll
            for (int qt = 0; qt < 2; ++qt)
#pragma unroll
                for (int n = 0; n < 4; ++n) *(LDSP f32x4*)(lds + (wid * 8 + qt * 4 + n) * 1024 + lane * 16) = c[qt][n];
            RAW_BARRIER();
            f32x4 s = {0.f, 0.f, 0.f, 0.f};
#pragma unroll
            for (int w = 0; w < 8; ++w) s += *(LDSP f32x4*)(lds + (w * 8 + wid) * 1024 + lane * 16);
            u32x2 o;
            o.x = pack2(blo(xv.x) + bfr(bfr(s[0]) * blo(gv.x)), bhi(xv.x) + bfr(bfr(s[1]) * bhi(gv.x)));
            o.y = pack2(blo(xv.y) + bfr(bfr(s[2]) * blo(gv.y)), bhi(xv.y) + bfr(bfr(s[3]) * bhi(gv.y)));
            *(u32x2*)(a.out + eo) = o;
        }
    }
}

#define BL4 "buffer_load_dwordx4 "
#define SCHED() __builtin_amdgcn_sched_barrier(0)
template <int N> struct IC { static constexpr int value = N; };
DEV u32 umax3(u32 a, u32 b, u32 c) { return umax(umax(a, b), c); }
DEV u32x4 bld128so(rsrc_t r, u32 vo, u32 so) { return __builtin_amdgcn_raw_buffer_load_b128(r, vo, so, 16); }
DEV float dpp_ror8(float v) { return bitsf((u32)__builtin_amdgcn_update_dpp(0, (int)fbits(v), 0x128, 0xf, 0xf, true)); }
DEV int memi_unit(int k) { return k ^ (((k >> 4) & 1) << 3); }

constexpr int L_MEMI = 0, L_QKI = 10240, L_RING = 16384;

DEV int gather_mem(const Args& a, u32x4 rs, lptr lds, int s, u32 so, u32 tshot, u32 base, int wid, int lane, int tid, bool& dead) {
    int n = 0;
    if (s == 0) {
        for (int k = tid; k < DM; k += 512) {
            const unsigned short* m = (const unsigned short*)a.mem0 + k;
            lptr d = lds + L_MEMI + (k >> 4) * 256 + (((k & 15) * 8) ^ (((k >> 4) & 1) * 64));
            *(LDSP u32x2*)d = (u32x2){m[0] | ((u32)m[DM] << 16), m[2 * DM] | ((u32)m[3 * DM] << 16)};
            *(LDSP u32x2*)(d + 128) = (u32x2){m[4 * DM] | ((u32)m[5 * DM] << 16), m[6 * DM] | ((u32)m[7 * DM] << 16)};
        }
    } else if (wid >= 3) {
        u32x4 t0, t1;
        lane = opq(lane);
        const u32 o = opq((wid - 3) * 2048 + lane * 16);
        const u32 v0 = opq(blk(o));
        wait_slot(tshot, base);
        asm volatile("s_nop 4\n\t" BL4 "%0, %2, %3, %4 offen sc1\n\t" BL4 "%1, %2, %3, %4 offen offset:1024 sc1\n\ts_waitcnt vmcnt(0)"
                     : "=&v"(t0), "=&v"(t1) : "v"(v0), "s"(rs), "s"(so) : "memory");
        for (;;) {
            *(LDSP u32x4*)(lds + L_MEMI + o) = t0;
            *(LDSP u32x4*)(lds + L_MEMI + o + 1024) = t1;
            SCHED();
            const unsigned long long miss = __ballot(umax(max4(t0), max4(t1)) == SENT);
            if (__builtin_expect(miss == 0, 1) || dead) break;
            asm volatile("s_waitcnt lgkmcnt(0)\n\ts_mov_b64 exec, %5\n\ts_nop 4\n\t" BL4 "%0, %2, %3, %4 offen sc1\n\t" BL4 "%1, %2, %3, %4 offen offset:1024 sc1\n\t"
                         "s_mov_b64 exec, -1\n\ts_waitcnt vmcnt(0)"
                         : "+v"(t0), "+v"(t1) : "v"(v0), "s"(rs), "s"(so), "s"(miss) : "memory");
            if (++n > SPIN_CAP) dead = true;
        }
    }
    return n;
}
DEV u32 get_parity(rsrc_t rws, u32* flags, lptr lds, bool prober, int wid, int lane, bool& dead) {
    if (prober) {
        if (wid < 4) {
            u32 best = ~0u;
            for (int k = 0; k < 12; ++k) {
                const u32 c0 = cyc();
                WAIT_L0();
                if (lane == 0) bst32<16>(rws, SENT, OFF_X + (wid >> 1) * 2 * MB + (wid & 1) * 8192u + k * 65536u + 16384u + 2048u);
                WAIT_V(0);
                const u32 d = cyc() - c0;
                best = d < best ? d : best;
            }
            if (lane == 0) ((LDSP u32*)lds)[wid] = best;
        }
        RAW_BARRIER();
        if (wid == 0 && lane == 0) {
            LDSP u32* r = (LDSP u32*)lds;
            const u32 v = ((u32)__builtin_amdgcn_s_memrealtime() & ~15u) | 8u | (r[1] < r[0] ? 1u : 0u) | (r[3] < r[2] ? 2u : 0u);
            __hip_atomic_store(flags + F_PAR * 32, v, __ATOMIC_RELAXED, __HIP_MEMORY_SCOPE_AGENT);
        }
        RAW_BARRIER();
    }
    wait_flag(flags, F_PAR, 8, dead);
    return __builtin_amdgcn_readfirstlane(__hip_atomic_load(flags + F_PAR * 32, __ATOMIC_RELAXED, __HIP_MEMORY_SCOPE_AGENT));
}
DEV int a_off(int lane) {
    const int G = lane >> 4, q = (lane >> 2) & 3, p = lane & 3;
    return 16 * memi_unit(16 * G + 4 * (q >> 1) + (q & 1) + 2 * (p >> 1)) + 8 * (p & 1);
}
DEV int a_off2(int lane) {
    const int G = lane >> 4, q = (lane >> 2) & 3, p = lane & 3;
    return G * 256 + (p & 1) * 128 + (((4 * (q >> 1) + (q & 1) + 2 * (p >> 1)) * 8) ^ ((G & 1) * 64));
}
DEV void stage_rows(u32x4 r, u32 off, int ld, int rowb, int rows, lptr dst, int wid, int lane) {
    const int total = rows * rowb / 1024;
    for (int i = wid; i < total; i += 8) {
        const int b = i * 1024 + lane * 16;
        bdma<0>(r, (u32)(b / rowb) * ld + (b % rowb), off, (u32)(size_t)dst + i * 1024);
    }
}
DEV u32x8 wfrag(lptr img, int rowb, int k0, int col0, int lane) {
    const int g = lane >> 4, q = (lane >> 2) & 3, p = lane & 3;
    lptr b = img + (k0 + 8 * g + q) * rowb + (col0 + 4 * p) * 2;
    return cat(cat(trread(b), trread(b + 4 * rowb)), cat(trread(b + 32 * rowb), trread(b + 36 * rowb)));
}

DEV void chain_s(const Args& a, lptr lds, int h, int wid, int lane, int tid) {
    char* ws = a.ws;
    u32* flags = (u32*)(ws + OFF_FLAGS);
    const rsrc_t rws = __builtin_amdgcn_make_buffer_rsrc(ws, 0, WS_USED, 0x00020000);
    const u32x4 rs = mkrsrc(ws, WS_USED);
    const int g = lane >> 4, fr = lane & 15;
    bool dead = false;
    u32x8 W[2][10];
    {
        const u32x4 rq = mkrsrc(a.Wq, WQ_BYTES), rk = mkrsrc(a.Wk, WQ_BYTES);
        auto st = [&](int pc) {
            lptr d = lds + (pc & 1) * 65536;
            stage_rows(rq, (u32)pc * 128u * 8192u + h * 256, 8192, 256, 128, d, wid, lane);
            stage_rows(rk, (u32)pc * 128u * 8192u + h * 256, 8192, 256, 128, d + 32768, wid, lane);
        };
        st(0);
#pragma unroll
        for (int pc = 0; pc < 5; ++pc) {
            if (pc + 1 < 5) { st(pc + 1); WAIT_V(8); } else WAIT_V(0);
            RAW_BARRIER();
            lptr img = lds + (pc & 1) * 65536 + (wid >> 2) * 32768;
#pragma unroll
            for (int c = 0; c < 2; ++c)
#pragma unroll
                for (int t = 0; t < 2; ++t) W[t][pc * 2 + c] = wfrag(img, 256, c * 64, (wid & 3) * 32 + t * 16, lane);
            RAW_BARRIER();
        }
    }
    const u32 ldsb = (u32)(size_t)lds;
    auto dma = [&](int seg, int slot) {
        const u32 d = ldsb + L_RING + slot * 16384 + (wid - 1) * 8192;
        const int ln = opq(lane), f = ln & 15;
        const u32 vo = (wid == 1 ? 16 * ((f & 3) >> 1) + 8 * (f >> 3) + 2 * ((f >> 2) & 1) + (f & 1) : f) * 8192 + (ln >> 4) * 16;
        const u32 sb = (wid == 1 ? OFF_K : OFF_Q) + (u32)seg * 32u * 8192u + h * 256, ts = (wid == 1 ? 4 : 16) * 8192;
#pragma unroll
        for (int t = 0; t < 2; ++t)
#pragma unroll
            for (int c = 0; c < 4; ++c) bdma<1>(rs, vo, sb + t * ts + c * 64, d + (t * 4 + c) * 1024);
    };
    const u32 par = get_parity(rws, flags, lds, false, wid, lane, dead);
    if (wid == 1 || wid == 2) { wait_flag(flags, F_KQ + h, 1, dead); dma(0, 0); dma(1, 1); WAIT_V(0); }
    RAW_BARRIER();
    const int idx = (fr < 8) ? 0x4444 : 0xEEEE;
    const int ao = a_off2(lane);
    const float cs = 0.08838834764831845f * 1.4426950408889634f;
    TC_DECL;
    auto project = [&](auto nw) {
        constexpr int NW = decltype(nw)::value;
        RAW_BARRIER();
        TC(2);
        u32x4 av[10];
#pragma unroll
        for (int c = 0; c < NW; ++c) { av[c] = cat(trread(lds + L_MEMI + c * 1024 + ao), trread(lds + L_MEMI + c * 1024 + (ao ^ 64))); SCHED(); }
        f32x4 c0 = {0.f, 0.f, 0.f, 0.f}, c1 = c0;
        SCHED();
#pragma unroll
        for (int c = 0; c < 10; ++c) {
            c0 = smfmac(av[c], W[0][c], c0, idx);
            c1 = smfmac(av[c], W[1][c], c1, idx);
            SCHED();
            if (c + NW < 10) av[c + NW] = cat(trread(lds + L_MEMI + (c + NW) * 1024 + ao), trread(lds + L_MEMI + (c + NW) * 1024 + (ao ^ 64)));
            SCHED();
        }
        WAIT_V(0);
        const u32 d0 = pack2(fold32(c0[0], c1[0]), fold32(c0[1], c1[1])), d1 = pack2(fold32(c0[2], c1[2]), fold32(c0[3], c1[3]));
        LDSP u32* qp = (LDSP u32*)(lds + L_QKI + (wid * 32 + (lane >> 5) * 16 + fr) * 16 + (g & 1) * 4);
        qp[0] = d0; qp[2] = d1;
        RAW_BARRIER();
        TC(3);
    };
    gather_mem(a, rs, lds, 0, 0, 0, 0, wid, lane, tid, dead);
    if (wid >= 3) {
        u32 tshot = a.fs;
        for (int s = 0; s < NSEG; ++s) {
            TC(0);
            int nre = 0;
            if (s) nre = gather_mem(a, rs, lds, s, xstep(s, par), tshot, par, wid, lane, tid, dead);
            TCV(9, nre); TC(1);
            project(IC<7>());
            if (s) tshot = next_slot(tshot, a.T, a.gs, par);
            if (wid == 3 && h == 0 && s >= 3 && lane == 0) bst32<16>(rws, FX_FULL, OFF_FLAGS + (F_X + s - 3) * 128);
            TC_FLUSH(s);
        }
        TC_DUMP();
        return;
    }
    lptr qk = lds + L_QKI + (8 * g + ((lane >> 2) & 3)) * 16 + 8 * (lane & 1);
    int slot = 0;
    if (wid == 0) {
        const int qi = 4 * ((fr >> 1) & 1) + 2 * ((fr >> 2) & 1) + (fr & 1);
        const u32 vpx = blk(XP_PX + h * 512u) + (8 * g + qi) * 16 + (fr >> 3) * 8;
        const u32 vpm = blk(XP_PM + (h >> 1) * 256u) + qi * 32 + (g & 1) * 16 + (h & 1) * 8 + (g >> 1) * 4;
        u32x4 kx[8];
#pragma unroll
        for (int i = 0; i < 8; ++i) kx[i] = lds128(lds + L_RING + i * 1024 + opq(lane) * 16);
        for (int s = 0; s < NSEG; ++s) {
            const u32 so = xstep(s, par);
            project(IC<7>());
            if (s + 1 == NSEG) break;
            u32x4 qf[4], km[4];
#pragma unroll
            for (int c = 0; c < 4; ++c) {
                qf[c] = cat(trread(qk + c * 512), trread(qk + c * 512 + 64));
                km[c] = cat(trread(qk + 2048 + c * 512), trread(qk + 2048 + c * 512 + 64));
                SCHED();
            }
            f32x4 s0 = {0.f, 0.f, 0.f, 0.f}, s1 = s0, s2 = s0;
#pragma unroll
            for (int c = 0; c < 4; ++c) {
                s0 = mfma(kx[c], qf[c], s0);
                s1 = mfma(kx[4 + c], qf[c], s1);
                s2 = mfma(km[c], qf[c], s2);
                asm volatile("" : "+v"(s2));
                SCHED();
            }
            f32x2 x[3];
            x[0] = fr >= 8 ? (f32x2){s1[0], s1[1]} : (f32x2){s0[0], s0[1]};
            x[1] = fr >= 8 ? (f32x2){s1[2], s1[3]} : (f32x2){s0[2], s0[3]};
            x[2] = g >= 2 ? (f32x2){s2[2], s2[3]} : (f32x2){s2[0], s2[1]};
            float m, t;
            const f32x2 cs2 = {cs, cs};
            asm("v_max3_f32 %0, %5, %6, %7\n\tv_max3_f32 %1, %8, %9, %10\n\tv_max_f32 %0, %0, %1\n\tv_pk_mul_f32 %2, %2, %11\n\ts_nop 0\n\t"
                "v_max_f32_dpp %0, %0, %0 row_ror:8 row_mask:0xf bank_mask:0xf\n\tv_mov_b32 %1, %0\n\tv_pk_mul_f32 %3, %3, %11\n\ts_nop 0\n\t"
                "v_permlane32_swap_b32 %0, %1\n\tv_max_f32 %0, %0, %1\n\tv_mov_b32 %1, %0\n\tv_pk_mul_f32 %4, %4, %11\n\ts_nop 0\n\t"
                "v_permlane16_swap_b32 %0, %1\n\tv_max_f32 %0, %0, %1"
                : "=&v"(m), "=&v"(t), "+v"(x[0]), "+v"(x[1]), "+v"(x[2])
                : "v"(x[0].x), "v"(x[0].y), "v"(x[1].x), "v"(x[1].y), "v"(x[2].x), "v"(x[2].y), "s"(cs2));
            const float mc = m * cs;
#pragma unroll
            for (int r = 0; r < 3; ++r) {
                x[r] -= (f32x2){mc, mc};
                x[r].x = __builtin_amdgcn_exp2f(x[r].x); x[r].y = __builtin_amdgcn_exp2f(x[r].y);
            }
            const f32x2 ps = __builtin_elementwise_fma(x[2], (f32x2){0.5f, 0.5f}, x[0] + x[1]);
            float sum = ps.x + ps.y;
            sum += dpp_ror8(sum);
            sum = xsum(sum);
            const float inv = __builtin_amdgcn_rcpf(sum);
#pragma unroll
            for (int r = 0; r < 3; ++r) x[r] *= (f32x2){inv, inv};
            __builtin_amdgcn_raw_buffer_store_b64((u32x2){nosent(pack2(x[0].x, x[0].y)), nosent(pack2(x[1].x, x[1].y))}, rws, vpx, so, 16);
            __builtin_amdgcn_raw_buffer_store_b32(nosent(pack2(x[2].x, x[2].y)), rws, vpm, so, 16);
            TC(4);
            SCHED();
            slot = slot == 2 ? 0 : slot + 1;
#pragma unroll
            for (int i = 0; i < 8; ++i) kx[i] = lds128(lds + L_RING + slot * 16384 + i * 1024 + lane * 16);
            TC_FLUSH(s);
        }
        TC_DUMP();
        return;
    }
    u32x4 kx[8];
#pragma unroll
    for (int i = 0; i < 8; ++i) kx[i] = lds128(lds + L_RING + i * 1024 + opq(lane) * 16);
    for (int s = 0; s < NSEG; ++s) {
        project(IC<6>());
        const int ln = opq(lane), fr = ln & 15, g = ln >> 4;
        lptr rg = lds + L_RING + slot * 16384 + 8192 + (wid - 1) * 4096 + ln * 16;
        u32x4 qf[4], km[4];
#pragma unroll
        for (int c = 0; c < 4; ++c) {
            qf[c] = lds128(rg + c * 1024);
            km[c] = cat(trread(qk + 2048 + c * 512), trread(qk + 2048 + c * 512 + 64));
        }
        SCHED();
        f32x4 st[3];
#pragma unroll
        for (int t = 0; t < 3; ++t) st[t] = (f32x4){0.f, 0.f, 0.f, 0.f};
#pragma unroll
        for (int c = 0; c < 4; ++c) {
            st[0] = mfma(kx[c], qf[c], st[0]);
            st[1] = mfma(kx[4 + c], qf[c], st[1]);
            st[2] = mfma(km[c], qf[c], st[2]);
        }
        SCHED();
        const int pslot = slot == 0 ? 2 : slot - 1;
        slot = slot == 2 ? 0 : slot + 1;
#pragma unroll
        for (int i = 0; i < 8; ++i) kx[i] = lds128(lds + L_RING + slot * 16384 + i * 1024 + ln * 16);
        const float NEG = -3.0e38f;
        const int tq = (wid - 1) * 16 + fr;
#pragma unroll
        for (int t = 0; t < 2; ++t)
#pragma unroll
            for (int r = 0; r < 4; ++r) {
                const int key = 16 * (r >> 1) + 8 * (g >> 1) + 4 * t + 2 * (g & 1) + (r & 1);
                st[t][r] = key <= tq ? st[t][r] : NEG;
            }
        float m = fmaxf(fmaxf(fmaxf(st[0][0], st[0][1]), fmaxf(st[0][2], st[0][3])), fmaxf(fmaxf(st[1][0], st[1][1]), fmaxf(st[1][2], st[1][3])));
        m = fmaxf(m, fmaxf(fmaxf(st[2][0], st[2][1]), fmaxf(st[2][2], st[2][3])));
        m = xmax(m);
        float sum = 0.f;
#pragma unroll
        for (int t = 0; t < 3; ++t)
#pragma unroll
            for (int r = 0; r < 4; ++r) {
                const float e = __builtin_amdgcn_exp2f((st[t][r] - m) * cs);
                st[t][r] = e;
                sum += (t == 2 && g >= 2) ? 0.f : e;
            }
        sum = xsum(sum);
        const float inv = __builtin_amdgcn_rcpf(sum);
        u32 d[3][2];
#pragma unroll
        for (int t = 0; t < 3; ++t) { d[t][0] = pack2(st[t][0] * inv, st[t][1] * inv); d[t][1] = pack2(st[t][2] * inv, st[t][3] * inv); }
        swap16(d[0][0], d[1][0]); swap16(d[0][1], d[1][1]);
        bst128<16>(rws, (u32x4){d[0][0], d[1][0], d[0][1], d[1][1]}, OFF_PX + (u32)(s * 32 + h) * 2048u + (wid - 1) * 1024 + ln * 16);
        u32 e0 = d[2][0], e1 = d[2][1];
        swap16(d[2][0], e0); swap16(d[2][1], e1);
        if (g == 0) bst128<16>(rws, (u32x4){d[2][0], e0, d[2][1], e1}, OFF_PXM + (u32)(s * 8 + (h >> 2)) * 2048u + (wid - 1) * 1024 + ((h & 3) * 16 + fr) * 16);
        if (s >= NSEG - 3) {
            WAIT_V(0);
            if (lane == 0) flag_add(flags, F_X + s);
        }
        if (s + 2 < NSEG) {
            if (((s + 2) & 3) == 0) wait_flag(flags, F_KQ + ((s + 2) >> 2) * 32 + h, 1, dead);
            dma(s + 2, pslot);
        }
        TC_FLUSH(s);
    }
    TC_DUMP();
}

DEV void chain_v(const Args& a, lptr lds, int h, int wid, int lane, int tid) {
    char* ws = a.ws;
    u32* flags = (u32*)(ws + OFF_FLAGS);
    const rsrc_t rws = __builtin_amdgcn_make_buffer_rsrc(ws, 0, WS_USED, 0x00020000);
    const u32x4 rs = mkrsrc(ws, WS_USED);
    const int g = lane >> 4, fr = lane & 15;
    bool dead = false;
    u32x8 Wv[10], Wo[5][2];
    {
        const u32x4 rv = mkrsrc(a.Wv, WQ_BYTES), ro = mkrsrc(a.Wo, WQ_BYTES);
        auto stv = [&](int pc) { stage_rows(rv, (u32)pc * 320u * 8192u + h * 256, 8192, 256, 320, lds + (pc & 1) * 81920, wid, lane); };
        auto sto = [&](int pc) { stage_rows(ro, (u32)(h * 128 + pc * 64) * 1280u, 1280, 1280, 64, lds + (pc & 1) * 81920, wid, lane); };
        stv(0); stv(1);
#pragma unroll
        for (int pc = 0; pc < 2; ++pc) {
            WAIT_V(10);
            RAW_BARRIER();
#pragma unroll
            for (int c = 0; c < 5; ++c) Wv[pc * 5 + c] = wfrag(lds + pc * 81920, 256, c * 64, wid * 16, lane);
            RAW_BARRIER();
            sto(pc);
        }
#pragma unroll
        for (int pc = 0; pc < 2; ++pc) {
            if (pc == 0) WAIT_V(10); else WAIT_V(0);
            RAW_BARRIER();
#pragma unroll
            for (int t = 0; t < 5; ++t) Wo[t][pc] = wfrag(lds + pc * 81920, 1280, 0, wid * 80 + t * 16, lane);
        }
        RAW_BARRIER();
    }
    const u32 par = get_parity(rws, flags, lds, false, wid, lane, dead);
    u32 tshot = a.fv;
    TC_DECL;
    const int idx = (fr < 8) ? 0x4444 : 0xEEEE;
    const int ao = a_off(lane), ao2 = a_off2(lane);
    for (int s = 0; s < NSEG; ++s) {
        TC(0);
        if (s == 1) { wait_flag(flags, F_VB, 16, dead); wait_flag(flags, F_VB + 1, 16, dead); }
        const int nre = gather_mem(a, rs, lds, s, xstep(s, par), tshot, par, wid, lane, tid, dead);
        TCV(9, nre); TC(1);
        RAW_BARRIER();
        TC(2);
        {
            u32x4 av[10];
#pragma unroll
            for (int c = 0; c < 10; ++c) av[c] = cat(trread(lds + L_MEMI + c * 1024 + ao2), trread(lds + L_MEMI + c * 1024 + (ao2 ^ 64)));
            f32x4 c0 = {0.f, 0.f, 0.f, 0.f};
            SCHED();
#pragma unroll
            for (int c = 0; c < 10; ++c) { c0 = smfmac(av[c], Wv[c], c0, idx); SCHED(); }
            const u32 d = pack2(fold32(c0[0], c0[2]), fold32(c0[1], c0[3]));
            *(LDSP u32*)(lds + L_QKI + memi_unit(wid * 16 + fr) * 16 + (2 * (g & 1) + (g >> 1)) * 4) = d;
        }
        RAW_BARRIER();
        TC(3);
        {
            f32x4 u[5];
            u32x4 av[2];
#pragma unroll
            for (int c = 0; c < 2; ++c) av[c] = cat(trread(lds + L_QKI + c * 1024 + ao), trread(lds + L_QKI + c * 1024 + (ao ^ 128)));
#pragma unroll
            for (int t = 0; t < 5; ++t) u[t] = (f32x4){0.f, 0.f, 0.f, 0.f};
            SCHED();
#pragma unroll
            for (int c = 0; c < 2; ++c)
#pragma unroll
                for (int t = 0; t < 5; ++t) u[t] = smfmac(av[c], Wo[t][c], u[t], idx);
            SCHED();
            WAIT_V(0);
            u32 x0 = pack2(fold32(u[0][0], u[1][0]), fold32(u[0][1], u[1][1])), x1 = pack2(fold32(u[0][2], u[1][2]), fold32(u[0][3], u[1][3]));
            u32 y0 = pack2(fold32(u[2][0], u[3][0]), fold32(u[2][1], u[3][1])), y1 = pack2(fold32(u[2][2], u[3][2]), fold32(u[2][3], u[3][3]));
            swap16(x0, y0); swap16(x1, y1);
            const u32 ub = OFF_U + (u32)(s * 32 + h) * 10240u + wid * 1280;
            bst128<16>(rws, (u32x4){nosent(x0), nosent(x1), nosent(y0), nosent(y1)}, ub + (((g & 1) * 2 + (g >> 1)) * 16 + fr) * 16);
            u32 e0 = pack2(fold32(u[4][0], u[4][2]), fold32(u[4][1], u[4][3])), e1 = e0;
            swap32(e0, e1);
            u32 f0 = e0, f1 = e1;
            swap16(e0, f0); swap16(e1, f1);
            if (g == 0) bst128<16>(rws, (u32x4){nosent(e0), nosent(e1), nosent(f0), nosent(f1)}, ub + (64 + fr) * 16);
        }
        TC(4);
        if (s) tshot = next_slot(tshot, a.T, a.gv, par);
        if (s >= NSEG - 3) {
            WAIT_V(0);
            if (lane == 0) flag_add(flags, F_X + s);
        }
        TC_FLUSH(s);
    }
    TC_DUMP();
}

DEV void chain_c(const Args& a, lptr lds, int tile, int wid, int lane, int tid) {
    char* ws = a.ws;
    u32* flags = (u32*)(ws + OFF_FLAGS);
    const rsrc_t rws = __builtin_amdgcn_make_buffer_rsrc(ws, 0, WS_USED, 0x00020000);
    const u32x4 rs = mkrsrc(ws, WS_USED);
    const int g = lane >> 4, fr = lane & 15;
    bool dead = false;
    const u32 ldsb = (u32)(size_t)lds;
    const u32 par = get_parity(rws, flags, lds, tile == 0, wid, lane, dead);
    TC_DECL;
    if (wid >= 4) {
        for (int s = 0; s < NSEG - 1; ++s) {
            if ((s & 3) == 0) wait_flag(flags, F_VB + (s >> 2) * 2 + ((wid - 4) >> 1), 16, dead);
            const u32 sb = OFF_B + ((u32)(s * 32 + (wid - 4) * 8) * 40u + tile) * 1024u, d = ldsb + (s & 1) * 32768 + (wid - 4) * 8192;
#pragma unroll
            for (int i = 0; i < 8; ++i) bdma<1>(rs, lane * 16, sb + i * 40960, d + i * 1024);
            WAIT_V(0);
            RAW_BARRIER();
        }
    } else if (wid > 0) {
        for (int s = 0; s < NSEG - 1; ++s) RAW_BARRIER();
    } else {
        constexpr int NB = 24;
        const int idx = (fr < 8) ? 0x4444 : 0xEEEE;
        const u32 vx = lane * 16, vm = g * 256 + (fr & 7) * 32 + (fr >> 3) * 16, vu = (u32)g * 10240u + (tile * 16 + fr) * 16;
        const u32 vst = lane < 32 ? blk(tile * 256u) + g * 128 + ((fr * 8) ^ ((tile & 1) * 64)) : 0x80000000u;
        u32 tshot = a.fp;
        for (int s = 0; s < NSEG - 1; ++s) {
            RAW_BARRIER();
            TC(0);
            lptr rg = lds + (s & 1) * 32768 + lane * 16;
            u32x4 yb[32], p[20];
            u32x8 yw[4];
#pragma unroll
            for (int i = 0; i < NB; ++i) yb[i] = lds128(rg + i * 1024);
            const u32 so = xstep(s, par), su = OFF_U + (u32)s * 327680u;
            u32 sn = xstep(s + 1, par), sp[5] = {so + 0x5000u, so + 0x8000u, so + 0x9000u, so + 0xc000u, so + 0xd000u};
            asm volatile("" : "+s"(sn), "+s"(sp[0]), "+s"(sp[1]), "+s"(sp[2]), "+s"(sp[3]), "+s"(sp[4]));
            int nrp = 0;
            f32x4 c0 = {0.f, 0.f, 0.f, 0.f};
            asm volatile("" : "+v"(c0));
            SCHED();
            wait_slot(tshot - a.fu, par);
            SCHED();
#pragma unroll
            for (int j = 0; j < 4; ++j) yw[j] = cat(bld128so(rws, vu, su + j * 81920), bld128so(rws, vu, su + j * 81920 + 40960));
            SCHED();
            TC(1);
            wait_slot(tshot, par);
            TC(3);
            SCHED();
#pragma unroll
            for (int i = 0; i < 20; ++i) { p[i] = bld128so(rws, (i < 16 ? vx : vm) + (i & 3) * 1024, sp[i >> 2]); SCHED(); }
            u32 mx = 0, mu = 0;
#pragma unroll
            for (int j = 0; j < 16; ++j) mu = umax3(mu, yw[j >> 2][(j & 3) * 2], yw[j >> 2][(j & 3) * 2 + 1]);
            SCHED();
            TC(4);
            SCHED();
#pragma unroll
            for (int i = 0; i < 20; ++i) {
                c0 = smfmac(p[i], i < 16 ? cat(yb[2 * (i & 15)], yb[2 * (i & 15) + 1]) : yw[i & 3], c0, idx);
                mx = umax3(umax3(mx, p[i].x, p[i].y), p[i].z, p[i].w);
                if (i == 0) TC(5);
                if (i == 15) TC(6);
                if (i < (32 - NB) / 2) { yb[NB + 2 * i] = lds128(rg + (NB + 2 * i) * 1024); yb[NB + 2 * i + 1] = lds128(rg + (NB + 2 * i + 1) * 1024); }
                SCHED();
            }
            if (__builtin_expect(__ballot(umax(mx, mu) == SENT) != 0, 0)) {
                TCV(7, __ballot(mx == SENT) != 0); TCV(8, __ballot(mu == SENT) != 0);
                u32x4 q[20], yu[8];
#pragma unroll
                for (int i = 0; i < 20; ++i) asm volatile("" : "=v"(q[i]));
#pragma unroll
                for (int j = 0; j < 8; ++j) asm volatile("" : "=v"(yu[j]));
                unsigned long long miss = ~0ull;
                for (;;) {
#define UL(j, o) BL4 "%" #j ", %8, %9, %" #o " offen sc1\n\t"
                    asm volatile("s_mov_b64 exec, %10\n\ts_nop 4\n\t" UL(0, 11) UL(1, 12) UL(2, 13) UL(3, 14) UL(4, 15) UL(5, 16) UL(6, 17) UL(7, 18) "s_mov_b64 exec, -1"
                                 : "+v"(yu[0]), "+v"(yu[1]), "+v"(yu[2]), "+v"(yu[3]), "+v"(yu[4]), "+v"(yu[5]), "+v"(yu[6]), "+v"(yu[7])
                                 : "v"(vu), "s"(rs), "s"(miss), "s"(su), "s"(su + 40960u), "s"(su + 81920u), "s"(su + 122880u), "s"(su + 163840u),
                                   "s"(su + 204800u), "s"(su + 245760u), "s"(su + 286720u) : "memory");
#define PL(j, v, o, f) BL4 "%" #j ", %" #v ", %22, %" #o " offen offset:" #f " sc1\n\t"
#define PL4(a, b, c, d, v, o) PL(a, v, o, 0) PL(b, v, o, 1024) PL(c, v, o, 2048) PL(d, v, o, 3072)
                    asm volatile("s_mov_b64 exec, %23\n\ts_nop 4\n\t" PL4(0, 1, 2, 3, 20, 24) PL4(4, 5, 6, 7, 20, 25) PL4(8, 9, 10, 11, 20, 26)
                                 PL4(12, 13, 14, 15, 20, 27) PL4(16, 17, 18, 19, 21, 28) "s_mov_b64 exec, -1\n\ts_waitcnt vmcnt(0)"
                                 : "+v"(q[0]), "+v"(q[1]), "+v"(q[2]), "+v"(q[3]), "+v"(q[4]), "+v"(q[5]), "+v"(q[6]), "+v"(q[7]), "+v"(q[8]), "+v"(q[9]),
                                   "+v"(q[10]), "+v"(q[11]), "+v"(q[12]), "+v"(q[13]), "+v"(q[14]), "+v"(q[15]), "+v"(q[16]), "+v"(q[17]), "+v"(q[18]), "+v"(q[19])
                                 : "v"(vx), "v"(vm), "s"(rs), "s"(miss), "s"(sp[0]), "s"(sp[1]), "s"(sp[2]), "s"(sp[3]), "s"(sp[4]) : "memory");
                    asm volatile("" : "+v"(yu[0]), "+v"(yu[1]), "+v"(yu[2]), "+v"(yu[3]), "+v"(yu[4]), "+v"(yu[5]), "+v"(yu[6]), "+v"(yu[7]));
                    mx = 0;
#pragma unroll
                    for (int i = 0; i < 20; ++i) mx = umax3(umax3(mx, q[i].x, q[i].y), q[i].z, q[i].w);
#pragma unroll
                    for (int j = 0; j < 8; ++j) mx = umax3(umax3(mx, yu[j].x, yu[j].y), yu[j].z, yu[j].w);
                    miss = __ballot(mx == SENT);
                    if (miss == 0 || dead) break;
                    if (++nrp > SPIN_CAP) dead = true;
                }
                c0 = (f32x4){0.f, 0.f, 0.f, 0.f};
#pragma unroll
                for (int i = 0; i < 20; ++i)
                    c0 = smfmac(q[i], i < 16 ? cat(lds128(rg + 2 * (i & 15) * 1024), lds128(rg + (2 * (i & 15) + 1) * 1024)) : cat(yu[2 * (i & 3)], yu[2 * (i & 3) + 1]), c0, idx);
            } else { TCV(7, 0); TCV(8, 0); }
            u32 e0 = nosent(pack2(fold32(c0[0], c0[2]), fold32(c0[1], c0[3]))), e1 = e0;
            swap32(e0, e1);
            __builtin_amdgcn_raw_buffer_store_b64((u32x2){e0, e1}, rws, vst, sn, 16);
            TC(2);
            tshot = next_slot(tshot, a.T, s ? a.gc : a.gc + 200, par);
            TCV(9, tc[7]); TCV(7, 0);
            TC_FLUSH(s);
        }
    }
    TC_DUMP();
}

__global__ void __launch_bounds__(512, 2) main_kernel(Args a) {
    __shared__ __attribute__((aligned(1024))) char smem[163840];
    lptr lds = (lptr)smem;
    const int tid = threadIdx.x, lane = tid & 63, wid = __builtin_amdgcn_readfirstlane(tid >> 6);
    a.ws = ws_base(a.ws);
    const int x = blockIdx.x & 7, t = blockIdx.x >> 3;
    if (x >= 4) worker(a, lds, (LDSP int*)(lds + 163824), wid, lane, tid);
    else if (t < 8) chain_s(a, lds, x * 8 + t, wid, lane, tid);
    else if (t < 16) chain_v(a, lds, x * 8 + t - 8, wid, lane, tid);
    else if (t < 26) chain_c(a, lds, x * 10 + t - 16, wid, lane, tid);
}

extern "C" void kernel_launch(void* const* d_in, const int* in_sizes, int n_in, void* d_out, int out_size, void* d_ws, size_t ws_size,
                              hipStream_t stream) {
    Args a;
    a.x = (const char*)d_in[0]; a.mem0 = (const char*)d_in[1]; a.Wq = (const char*)d_in[2]; a.Wk = (const char*)d_in[3];
    a.Wv = (const char*)d_in[4]; a.Wo = (const char*)d_in[5]; a.w1 = (const char*)d_in[8]; a.b1 = (const char*)d_in[9];
    a.w2 = (const char*)d_in[10]; a.b2 = (const char*)d_in[11];
    a.out = (char*)d_out; a.ws = (char*)d_ws;
    a.T = 880; a.fs = 0; a.fv = 0; a.fp = 560; a.fu = 100;
    a.gs = a.T - 480; a.gv = a.T - 620; a.gc = a.T - 380;
    prologue_kernel<<<512, 256, 0, stream>>>((const u32*)d_in[0], (char*)d_ws);
    main_kernel<<<256, 512, 0, stream>>>(a);
}
```

```cpp
#include <hip/hip_runtime.h>
#include <hip/hip_bf16.h>
#include <stdint.h>
#pragma clang diagnostic ignored "-Winline-asm"

typedef unsigned u32;
typedef u32 u32x2 __attribute__((ext_vector_type(2)));
typedef u32 u32x4 __attribute__((ext_vector_type(4)));
typedef u32 u32x8 __attribute__((ext_vector_type(8)));
typedef float f32x4 __attribute__((ext_vector_type(4)));
typedef __bf16 bf16x2 __attribute__((ext_vector_type(2)));
typedef __bf16 bf16x8 __attribute__((ext_vector_type(8)));
typedef __bf16 bf16x16 __attribute__((ext_vector_type(16)));
typedef short s16x4 __attribute__((ext_vector_type(4)));
#define LDSP __attribute__((address_space(3)))
typedef LDSP char* lptr;
#define DEV __device__ __forceinline__

constexpr int SEQ = 2048, DM = 640, NH = 32, DH = 128, NSEG = 64, FF = 1728;
constexpr u32 SENT = 0xFFFFFFFFu;
constexpr u32 WQ_BYTES = 640u * 4096u * 2u, W1_BYTES = 640u * 1728u * 2u;
constexpr int SPIN_CAP = 400000;

constexpr u32 MB = 1u << 20;
constexpr u32 OFF_FLAGS = 0;
constexpr u32 FLAG_BYTES = 128 * 1024;
constexpr u32 OFF_X = 2 * MB;
constexpr u32 OFF_U = 8 * MB;
constexpr u32 SENT_END = OFF_U + 64u * 32u * 10240u;
constexpr u32 OFF_XN = 32 * MB;
constexpr u32 OFF_G = 36 * MB;
constexpr u32 OFF_H = 40 * MB;
constexpr u32 OFF_PX = 48 * MB;
constexpr u32 OFF_PXM = 54 * MB;
constexpr u32 OFF_Q = 56 * MB;
constexpr u32 OFF_K = 76 * MB;
constexpr u32 OFF_B = 96 * MB;
constexpr u32 WS_USED = 176 * MB;
constexpr int F_QX = 0, F_QG = 8, F_PAR = 9, F_VB = 16  , F_H = 48  , F_G = 64  , F_X = 96  , F_KQ = 256  ;

#define TC_DECL do {} while (0)
#define TC(k) do {} while (0)
#define TCV(k, v) do {} while (0)
#define TC_FLUSH(s) do {} while (0)
#define TC_DUMP() do {} while (0)
#define TS(slot, k) do {} while (0)
#define TSV(slot, k, v) do {} while (0)
#define WAIT_V(n) asm volatile("s_waitcnt vmcnt(%0)" ::"n"(n) : "memory")
#define WAIT_L0() asm volatile("s_waitcnt lgkmcnt(0)" ::: "memory")
#define RAW_BARRIER() do { WAIT_L0(); __builtin_amdgcn_s_barrier(); asm volatile("" ::: "memory"); } while (0)

DEV u32 pack2(float a, float b) { bf16x2 v = {(__bf16)a, (__bf16)b}; return __builtin_bit_cast(u32, v); }
DEV float bfr(float a) { return (float)(__bf16)a; }
DEV float blo(u32 d) { return __builtin_bit_cast(float, d << 16); }
DEV float bhi(u32 d) { return __builtin_bit_cast(float, d & 0xffff0000u); }
DEV u32 fbits(float f) { return __builtin_bit_cast(u32, f); }
DEV float bitsf(u32 u) { return __builtin_bit_cast(float, u); }
DEV u32 nosent(u32 d) { return d < 0xFFFFFFFEu ? d : 0xFFFFFFFEu; }
DEV void swap32(u32& a, u32& b) { auto r = __builtin_amdgcn_permlane32_swap(a, b, false, false); a = r[0]; b = r[1]; }
DEV void swap16(u32& a, u32& b) { auto r = __builtin_amdgcn_permlane16_swap(a, b, false, false); a = r[0]; b = r[1]; }
DEV float fold32(float a, float b) { u32 x = fbits(a), y = fbits(b); swap32(x, y); return bitsf(x) + bitsf(y); }
DEV float xmax(float v) {
    u32 a = fbits(v), b = a; swap32(a, b); v = fmaxf(bitsf(a), bitsf(b));
    a = fbits(v); b = a; swap16(a, b); return fmaxf(bitsf(a), bitsf(b));
}
DEV float xsum(float v) {
    u32 a = fbits(v), b = a; swap32(a, b); v = bitsf(a) + bitsf(b);
    a = fbits(v); b = a; swap16(a, b); return bitsf(a) + bitsf(b);
}
DEV u32x2 trread(lptr p) { s16x4 v = __builtin_amdgcn_ds_read_tr16_b64_v4i16((LDSP s16x4*)p); return __builtin_bit_cast(u32x2, v); }
DEV u32x4 lds128(lptr p) { return *(LDSP u32x4*)p; }
DEV u32x4 cat(u32x2 a, u32x2 b) { return (u32x4){a.x, a.y, b.x, b.y}; }
DEV u32x8 cat(u32x4 a, u32x4 b) { return (u32x8){a.x, a.y, a.z, a.w, b.x, b.y, b.z, b.w}; }
DEV f32x4 mfma(u32x4 a, u32x4 b, f32x4 c) {
    return __builtin_amdgcn_mfma_f32_16x16x32_bf16(__builtin_bit_cast(bf16x8, a), __builtin_bit_cast(bf16x8, b), c, 0, 0, 0);
}
DEV f32x4 smfmac(u32x4 a, u32x8 b, f32x4 c, int idx) {
    return __builtin_amdgcn_smfmac_f32_16x16x64_bf16(__builtin_bit_cast(bf16x8, a), __builtin_bit_cast(bf16x16, b), c, idx, 0, 0);
}
template <int SC1> DEV void bdma(u32x4 rs, u32 vo, u32 so, u32 lo) {
    if (SC1) asm volatile("s_mov_b32 m0, %3\n\ts_nop 3\n\tbuffer_load_dwordx4 %0, %1, %2 offen sc1 lds" ::"v"(vo), "s"(rs), "s"(so), "s"(lo) : "memory", "m0");
    else asm volatile("s_mov_b32 m0, %3\n\ts_nop 3\n\tbuffer_load_dwordx4 %0, %1, %2 offen lds" ::"v"(vo), "s"(rs), "s"(so), "s"(lo) : "memory", "m0");
}
DEV u32x4 mkrsrc(const void* p, u32 bytes) {
    unsigned long long a = (unsigned long long)p;
    u32x4 r = {(u32)a, (u32)(a >> 32) & 0xffffu, bytes, 0x00020000u};
    r.x = __builtin_amdgcn_readfirstlane(r.x); r.y = __builtin_amdgcn_readfirstlane(r.y);
    r.z = __builtin_amdgcn_readfirstlane(r.z); r.w = __builtin_amdgcn_readfirstlane(r.w);
    return r;
}
DEV char* ws_base(char* ws) { return (char*)(((unsigned long long)ws + (2 * MB - 1)) & ~(unsigned long long)(2 * MB - 1)); }
DEV u32 blk(u32 o) { return (o >> 13) * 16384u + (o & 8191u); }
DEV u32 xstep(int s, u32 par) { const u32 r = (s >= 48) ? 2u : (s >= 24 ? 1u : 0u); return OFF_X + r * (2 * MB - 24u * 81920u) + (u32)s * 81920u + ((par >> r) & 1u) * 8192u; }
DEV int opq(int v) { asm volatile("" : "+v"(v)); return v; }
DEV u32 cyc() { return (u32)__builtin_amdgcn_s_memtime(); }
DEV void wait_cyc(u32 T) {
    int rem;
    for (;;) {
        rem = (int)(T - cyc());
        if (rem < 170 || rem > 20000) break;
        __builtin_amdgcn_s_sleep(1);
    }
#pragma unroll
    for (int i = 0; i < 6; ++i)
        if (rem >= 56 + 22 * i && rem < 170) asm volatile("s_nop 15");
}
DEV u32 max4(u32x4 v) { u32 a = v.x > v.y ? v.x : v.y, b = v.z > v.w ? v.z : v.w; return a > b ? a : b; }
DEV u32 umax(u32 a, u32 b) { return a > b ? a : b; }

typedef __amdgpu_buffer_rsrc_t rsrc_t;
template <int AUX> DEV void bst128(rsrc_t r, u32x4 v, u32 off) { __builtin_amdgcn_raw_buffer_store_b128(v, r, off, 0, AUX); }
template <int AUX> DEV void bst64(rsrc_t r, u32x2 v, u32 off) { __builtin_amdgcn_raw_buffer_store_b64(v, r, off, 0, AUX); }
template <int AUX> DEV void bst32(rsrc_t r, u32 v, u32 off) { __builtin_amdgcn_raw_buffer_store_b32(v, r, off, 0, AUX); }
DEV u32x4 bld128s(rsrc_t r, u32 off) { return __builtin_amdgcn_raw_buffer_load_b128(r, off, 0, 16); }

DEV void wait_flag(u32* flags, int idx, u32 target, bool& dead) {
    if (dead) return;
    int spins = 0;
    for (;;) {
        u32 v = __hip_atomic_load(flags + idx * 32, __ATOMIC_RELAXED, __HIP_MEMORY_SCOPE_AGENT);
        if (__builtin_amdgcn_readfirstlane(v) >= target) break;
        if (++spins > SPIN_CAP) { dead = true; break; }
        __builtin_amdgcn_s_sleep(8);
    }
    asm volatile("" ::: "memory");
}
DEV void flag_add(u32* flags, int idx) { __hip_atomic_fetch_add(flags + idx * 32, 1u, __ATOMIC_RELAXED, __HIP_MEMORY_SCOPE_AGENT); }

__global__ void __launch_bounds__(256) prologue_kernel(const u32* __restrict__ x, char* __restrict__ ws0) {
    char* ws = ws_base(ws0);
    const int tid = threadIdx.x, lane = tid & 63, wid = tid >> 6;
    const int row = blockIdx.x * 4 + wid;
    u32 d[5];
#pragma unroll
    for (int i = 0; i < 5; ++i) d[i] = x[row * 320 + i * 64 + lane];
    {
        const u32x4 s = {SENT, SENT, SENT, SENT};
        constexpr u32 n16 = (SENT_END - OFF_X) / 16;
        u32x4* p = (u32x4*)(ws + OFF_X);
        for (u32 i = blockIdx.x * 256 + tid; i < n16; i += 512 * 256) p[i] = s;
        const u32x4 z = {0, 0, 0, 0};
        u32 j = blockIdx.x * 256 + tid;
        if (j < FLAG_BYTES / 16) ((u32x4*)(ws + OFF_FLAGS))[j] = z;
    }
    float v[10], s = 0.f;
#pragma unroll
    for (int i = 0; i < 5; ++i) { v[2 * i] = blo(d[i]); v[2 * i + 1] = bhi(d[i]); s += v[2 * i] + v[2 * i + 1]; }
#pragma unroll
    for (int o = 32; o; o >>= 1) s += __shfl_xor(s, o);
    const float mean = s * (1.0f / DM);
    float q = 0.f;
#pragma unroll
    for (int i = 0; i < 10; ++i) { float t = v[i] - mean; q += t * t; }
#pragma unroll
    for (int o = 32; o; o >>= 1) q += __shfl_xor(q, o);
    const float mb = bfr(mean), vb = bfr(q * (1.0f / DM));
    const float den = bfr(sqrtf(bfr(vb + 1e-5f)));
    u32* xn = (u32*)(ws + OFF_XN);
#pragma unroll
    for (int i = 0; i < 5; ++i) xn[row * 320 + i * 64 + lane] = pack2(bfr(v[2 * i] - mb) / den, bfr(v[2 * i + 1] - mb) / den);
}

template <int NT>
DEV void gemm_core(f32x4 (&acc)[4][NT], u32x4 rX, u32 xoff, int ldx, u32x4 rW0, u32x4 rW1, u32 woff0, u32 woff1, int ldw, int chmax1, int nkt,
                   lptr lds, int wid, int lane) {
    constexpr int NP = NT / 2, STAGE = 16384 * (1 + NP), G = 2 + 2 * NP;
    const int sb = lane * 16, swz = sb ^ (((sb >> 9) & 1) << 5);
    const u32 xv = (u32)(16 * wid + (swz >> 6)) * ldx + (swz & 63);
    u32 wv0[2], wv1[2];
#pragma unroll
    for (int i = 0; i < 2; ++i) {
        const int row = 8 * wid + 4 * i + (lane >> 4);
        const int ch = (lane & 15) ^ (((row & 3) << 2) | ((row >> 2) & 3));
        wv0[i] = (u32)row * ldw + ch * 16;
        wv1[i] = (u32)row * ldw + (ch < chmax1 ? ch : chmax1) * 16;
    }
    const u32 ldsb = (u32)(size_t)lds;
    auto issue = [&](int kt, int buf) {
        const u32 b = ldsb + buf * STAGE;
        bdma<1>(rX, xv, xoff + kt * 128, b + wid * 2048);
        bdma<1>(rX, xv, xoff + kt * 128 + 64, b + wid * 2048 + 1024);
        const u32 wk = (u32)kt * 64u * ldw;
#pragma unroll
        for (int i = 0; i < 2; ++i) {
            bdma<0>(rW0, wv0[i], woff0 + wk, b + 16384 + (8 * wid + 4 * i) * 256);
            if (NP == 2) bdma<0>(rW1, wv1[i], woff1 + wk, b + 32768 + (8 * wid + 4 * i) * 256);
        }
    };
    const int wr = wid >> 2, wc = wid & 3, g = lane >> 4, q = (lane >> 2) & 3, p = lane & 3;
    const int ob = (lane & 15) * 64 + g * 16;
    const int xo = wr * 8192 + (ob ^ (((ob >> 9) & 1) << 5));
    int wo[NT][2];
#pragma unroll
    for (int n = 0; n < NT; ++n)
#pragma unroll
        for (int t = 0; t < 2; ++t) {
            const int panel = (NT == 4) ? (wc >> 1) : 0;
            const int c = (NT == 4) ? ((wc & 1) * 4 + n) : (wc * 2 + n);
            wo[n][t] = 16384 * (1 + panel) + 256 * (8 * g + 4 * t + q) + 16 * ((2 * c + (p >> 1)) ^ ((q << 2) | ((2 * g + t) & 3))) + 8 * (p & 1);
        }
#pragma unroll
    for (int m = 0; m < 4; ++m)
#pragma unroll
        for (int n = 0; n < NT; ++n) acc[m][n] = (f32x4){0.f, 0.f, 0.f, 0.f};
    issue(0, 0);
    issue(1, 1);
    int buf = 0;
    for (int kt = 0; kt < nkt; ++kt) {
        if (kt + 1 < nkt) WAIT_V(G); else WAIT_V(0);
        RAW_BARRIER();
        if (kt + 2 < nkt) issue(kt + 2, buf >= 1 ? buf - 1 : 2);
        lptr b = lds + buf * STAGE;
#pragma unroll
        for (int ks = 0; ks < 2; ++ks) {
            u32x4 xf[4], wf[NT];
#pragma unroll
            for (int m = 0; m < 4; ++m) xf[m] = lds128(b + xo + (m * 2 + ks) * 1024);
#pragma unroll
            for (int n = 0; n < NT; ++n) wf[n] = cat(trread(b + wo[n][0] + ks * 8192), trread(b + wo[n][1] + ks * 8192));
#pragma unroll
            for (int m = 0; m < 4; ++m)
#pragma unroll
                for (int n = 0; n < NT; ++n) acc[m][n] = mfma(wf[n], xf[m], acc[m][n]);
        }
        buf = (buf == 2) ? 0 : buf + 1;
    }
}
DEV u32x4 pair8(u32 a0, u32 a1, u32 b0, u32 b1) { swap16(a0, b0); swap16(a1, b1); return (u32x4){a0, a1, b0, b1}; }

struct Args {
    const char *x, *mem0, *Wq, *Wk, *Wv, *Wo, *w1, *b1, *w2, *b2;
    char *out, *ws;
    int ds, dv, dp, du;
};

DEV void worker(const Args& a, lptr lds, LDSP int* bc, int wid, int lane, int tid) {
    char* ws = a.ws;
    u32* flags = (u32*)(ws + OFF_FLAGS);
    const rsrc_t rws = __builtin_amdgcn_make_buffer_rsrc(ws, 0, WS_USED, 0x00020000);
    const u32x4 rs = mkrsrc(ws, WS_USED);
    const u32 ldsb = (u32)(size_t)lds;
    const int xq = (blockIdx.x & 7) - 2;
    const int wr = wid >> 2, wc = wid & 3, g = lane >> 4, fr = lane & 15;
    const int c8 = 8 * ((g >> 1) + 2 * (g & 1));
    int qi = 0;
    bool dead = false;
    int nit = 0;
    for (;;) {
        if (wid == 0 && nit < 80) TS(nit, 0);
        if (tid == 0) {
            int type = -1, p0 = 0, p1 = 0;
            while (qi < 6) {
                const int qq = (xq + qi) % 6, nq = qq < 4 ? 11 : 10;
                const int it = (int)__hip_atomic_fetch_add(flags + (F_QX + qq) * 32, 1u, __ATOMIC_RELAXED, __HIP_MEMORY_SCOPE_AGENT);
                if (it < 16 * nq) { const int u = qq + 6 * (it % nq); type = u >> 5 ^ 1; p0 = it / nq; p1 = u & 31; break; }
                ++qi;
            }
            if (type < 0) {
                const int it = (int)__hip_atomic_fetch_add(flags + F_QG * 32, 1u, __ATOMIC_RELAXED, __HIP_MEMORY_SCOPE_AGENT);
                if (it < 112) { type = 2; p0 = it / 7; p1 = it % 7; }
                else if (it < 192) { type = 3; p0 = (it - 112) / 5; p1 = (it - 112) % 5; }
                else if (it < 192 + 640) { type = 4; p0 = (it - 192) / 10; p1 = (it - 192) % 10; }
            }
            bc[0] = type; bc[1] = p0; bc[2] = p1;
        }
        RAW_BARRIER();
        const int type = __builtin_amdgcn_readfirstlane(bc[0]), p0 = __builtin_amdgcn_readfirstlane(bc[1]), p1 = __builtin_amdgcn_readfirstlane(bc[2]);
        RAW_BARRIER();
        if (type < 0) break;
        if (wid == 0 && nit < 80) { TS(nit, 1); TSV(nit, 2, type * 65536 + p0 * 256 + p1 + 1); }
        ++nit;
        if (type == 0 || type == 2) {
            f32x4 acc[4][4];
            {
                const bool qk = type == 0;
                const u32x4 r0 = mkrsrc(qk ? a.Wq : a.w1, qk ? WQ_BYTES : W1_BYTES), r1 = mkrsrc(qk ? a.Wk : a.w1, qk ? WQ_BYTES : W1_BYTES);
                const u32 o0 = qk ? p1 * 256 : p1 * 512, o1 = qk ? o0 : o0 + 256;
                gemm_core<4>(acc, rs, OFF_XN + (u32)p0 * 128u * 1280u, 1280, r0, r1, o0, o1, qk ? 8192 : FF * 2, (!qk && p1 == 6) ? 7 : 15, 10, lds, wid, lane);
            }
            if (type == 0) {
                const u32 base = ((wc >> 1) ? OFF_K : OFF_Q) + (u32)(p0 * 128 + wr * 64 + fr) * 8192u + p1 * 256 + ((wc & 1) * 64 + c8) * 2;
#pragma unroll
                for (int m = 0; m < 4; ++m)
#pragma unroll
                    for (int n = 0; n < 4; n += 2) {
                        u32x4 v = pair8(pack2(acc[m][n][0], acc[m][n][1]), pack2(acc[m][n][2], acc[m][n][3]),
                                        pack2(acc[m][n + 1][0], acc[m][n + 1][1]), pack2(acc[m][n + 1][2], acc[m][n + 1][3]));
                        bst128<16>(rws, v, base + m * 16 * 8192 + n * 32);
                    }
            } else {
                const int col0 = p1 * 256 + wc * 64;
                u32x2 bb[4];
#pragma unroll
                for (int n = 0; n < 4; ++n) { int c = col0 + n * 16 + 4 * g; c = c < FF - 4 ? c : FF - 4; bb[n] = *(const u32x2*)(a.b1 + c * 2); }
                const u32 base = OFF_H + (u32)(p0 * 128 + wr * 64 + fr) * (FF * 2) + (col0 + c8) * 2;
#pragma unroll
                for (int m = 0; m < 4; ++m) {
                    u32 h[4][2];
#pragma unroll
                    for (int n = 0; n < 4; ++n) {
                        float t0 = fmaxf(bfr(bfr(acc[m][n][0]) + blo(bb[n].x)), 0.f), t1 = fmaxf(bfr(bfr(acc[m][n][1]) + bhi(bb[n].x)), 0.f);
                        float t2 = fmaxf(bfr(bfr(acc[m][n][2]) + blo(bb[n].y)), 0.f), t3 = fmaxf(bfr(bfr(acc[m][n][3]) + bhi(bb[n].y)), 0.f);
                        h[n][0] = pack2(t0 * t0, t1 * t1); h[n][1] = pack2(t2 * t2, t3 * t3);
                    }
#pragma unroll
                    for (int n = 0; n < 4; n += 2) {
                        u32x4 v = pair8(h[n][0], h[n][1], h[n + 1][0], h[n + 1][1]);
                        if (col0 < FF) bst128<16>(rws, v, base + m * 16 * (FF * 2) + n * 32);
                    }
                }
            }
            WAIT_V(0);
            RAW_BARRIER();
            if (tid == 0) flag_add(flags, type == 0 ? F_KQ + p0 * 32 + p1 : F_H + p0);
        } else if (type == 1 || type == 3) {
            f32x4 acc[4][2];
            u32 wop[4];
            const u32x4 rWo = mkrsrc(a.Wo, WQ_BYTES);
            const u32 wob = (u32)p1 * 128u * 1280u;
            if (type == 1) {
#pragma unroll
                for (int i = 0; i < 4; ++i) {
                    const int row = 16 * wid + 4 * i + g;
                    const int ch = fr ^ (((row & 3) << 2) | ((row >> 2) & 3));
                    wop[i] = (u32)row * 1280u + ch * 16;
                    bdma<0>(rWo, wop[i], wob, ldsb + 3 * 32768 + (16 * wid + 4 * i) * 256);
                }
            } else {
                wait_flag(flags, F_H + p0, 7, dead);
            }
            {
                const bool v = type == 1;
                const u32x4 r0 = mkrsrc(v ? a.Wv : a.w2, v ? WQ_BYTES : W1_BYTES);
                gemm_core<2>(acc, rs, v ? OFF_XN + (u32)p0 * 128u * 1280u : OFF_H + (u32)p0 * 128u * (FF * 2), v ? 1280 : FF * 2, r0, r0, p1 * 256, 0,
                             v ? 8192 : 1280, 15, v ? 10 : 27, lds, wid, lane);
            }
            if (type == 1) {
                {
                    const int ob = fr * 64 + c8 * 2;
                    lptr vi = lds + 4 * 32768 + wc * 1024 + (ob ^ (((ob >> 9) & 1) << 5));
#pragma unroll
                    for (int m = 0; m < 4; ++m)
                        *(LDSP u32x4*)(vi + (wr * 4 + m) * 4096) = pair8(pack2(acc[m][0][0], acc[m][0][1]), pack2(acc[m][0][2], acc[m][0][3]),
                                                                          pack2(acc[m][1][0], acc[m][1][1]), pack2(acc[m][1][2], acc[m][1][3]));
                }
                RAW_BARRIER();
#pragma unroll
                for (int pn = 1; pn < 4; ++pn)
#pragma unroll
                    for (int i = 0; i < 4; ++i) bdma<0>(rWo, wop[i], wob + pn * 256, ldsb + (pn - 1) * 32768 + (16 * wid + 4 * i) * 256);
                u32x4 vf[4][4];
                {
                    const int ob = fr * 64 + g * 16;
                    lptr vi = lds + 4 * 32768 + wr * 16384 + (ob ^ (((ob >> 9) & 1) << 5));
#pragma unroll
                    for (int m = 0; m < 4; ++m)
#pragma unroll
                        for (int ks = 0; ks < 4; ++ks) vf[m][ks] = lds128(vi + m * 4096 + ks * 1024);
                }
                RAW_BARRIER();
#pragma unroll
                for (int i = 0; i < 4; ++i) bdma<0>(rWo, wop[i], wob + 4 * 256, ldsb + 4 * 32768 + (16 * wid + 4 * i) * 256);
                const int q = (lane >> 2) & 3, p = lane & 3;
                int wo[2][2];
#pragma unroll
                for (int n = 0; n < 2; ++n)
#pragma unroll
                    for (int t = 0; t < 2; ++t)
                        wo[n][t] = 256 * (8 * g + 4 * t + q) + 16 * ((2 * (wc * 2 + n) + (p >> 1)) ^ ((q << 2) | ((2 * g + t) & 3))) + 8 * (p & 1);
                for (int pn = 0; pn < 5; ++pn) {
                    if (pn) { WAIT_V(16); RAW_BARRIER(); }
                    lptr b = lds + (pn == 0 ? 3 : (pn == 4 ? 4 : pn - 1)) * 32768;
                    f32x4 c2[4][2];
#pragma unroll
                    for (int m = 0; m < 4; ++m) { c2[m][0] = (f32x4){0.f, 0.f, 0.f, 0.f}; c2[m][1] = c2[m][0]; }
#pragma unroll
                    for (int ks = 0; ks < 4; ++ks) {
                        u32x4 wf[2];
#pragma unroll
                        for (int n = 0; n < 2; ++n) wf[n] = cat(trread(b + wo[n][0] + ks * 8192), trread(b + wo[n][1] + ks * 8192));
#pragma unroll
                        for (int m = 0; m < 4; ++m)
#pragma unroll
                            for (int n = 0; n < 2; ++n) c2[m][n] = mfma(vf[m][ks], wf[n], c2[m][n]);
                    }
#pragma unroll
                    for (int sg = 0; sg < 2; ++sg)
#pragma unroll
                        for (int n = 0; n < 2; ++n) {
                            u32x4 v = {pack2(c2[2 * sg][n][0], c2[2 * sg][n][1]), pack2(c2[2 * sg][n][2], c2[2 * sg][n][3]),
                                       pack2(c2[2 * sg + 1][n][0], c2[2 * sg + 1][n][1]), pack2(c2[2 * sg + 1][n][2], c2[2 * sg + 1][n][3])};
                            const u32 seg = p0 * 4 + wr * 2 + sg;
                            bst128<16>(rws, v, OFF_B + ((seg * 32 + p1) * 40 + pn * 8 + wc * 2 + n) * 1024u + lane * 16);
                        }
                }
            } else {
                const int col0 = p1 * 128 + wc * 32;
                u32x2 bb[2];
#pragma unroll
                for (int n = 0; n < 2; ++n) bb[n] = *(const u32x2*)(a.b2 + (col0 + n * 16 + 4 * g) * 2);
                const u32 base = OFF_G + (u32)(p0 * 128 + wr * 64 + fr) * 1280u + (col0 + c8) * 2;
#pragma unroll
                for (int m = 0; m < 4; ++m) {
                    u32 h[2][2];
#pragma unroll
                    for (int n = 0; n < 2; ++n) {
                        float t[4] = {bfr(bfr(acc[m][n][0]) + blo(bb[n].x)), bfr(bfr(acc[m][n][1]) + bhi(bb[n].x)),
                                      bfr(bfr(acc[m][n][2]) + blo(bb[n].y)), bfr(bfr(acc[m][n][3]) + bhi(bb[n].y))};
#pragma unroll
                        for (int r = 0; r < 4; ++r) t[r] = t[r] / (1.0f + __expf(-t[r]));
                        h[n][0] = pack2(t[0], t[1]); h[n][1] = pack2(t[2], t[3]);
                    }
                    bst128<16>(rws, pair8(h[0][0], h[0][1], h[1][0], h[1][1]), base + m * 16 * 1280);
                }
            }
            WAIT_V(0);
            RAW_BARRIER();
            if (tid == 0) flag_add(flags, type == 1 ? F_VB + p0 * 2 + (p1 >> 4) : F_G + p0);
        } else {
            const int seg = p0, cg = p1;
            wait_flag(flags, F_VB + (seg >> 2) * 2 + (wid >> 2), 16, dead);
            u32x4 yb[4][4], yu[4], px[4][2], pm[2];
#pragma unroll
            for (int hh = 0; hh < 4; ++hh)
#pragma unroll
                for (int n = 0; n < 4; ++n) yb[hh][n] = bld128s(rws, OFF_B + ((seg * 32 + wid * 4 + hh) * 40 + cg * 4 + n) * 1024u + lane * 16);
            wait_flag(flags, F_G + (seg >> 2), 5, dead);
            const u32 eo = (u32)(seg * 32 + (wid >> 2) * 16 + fr) * 1280u + (cg * 64 + (wid & 3) * 16 + 4 * g) * 2;
            const u32x2 xv = *(const u32x2*)(a.x + eo);
            const u32x2 gv = __builtin_amdgcn_raw_buffer_load_b64(rws, OFF_G + eo, 0, 16);
            wait_flag(flags, F_X + seg, 96, dead);
#pragma unroll
            for (int hh = 0; hh < 4; ++hh)
#pragma unroll
                for (int qt = 0; qt < 2; ++qt) px[hh][qt] = bld128s(rws, OFF_PX + (seg * 32 + wid * 4 + hh) * 2048u + qt * 1024 + lane * 16);
#pragma unroll
            for (int qt = 0; qt < 2; ++qt) pm[qt] = bld128s(rws, OFF_PXM + (seg * 8 + wid) * 2048u + qt * 1024 + lane * 16);
#pragma unroll
            for (int n = 0; n < 4; ++n) yu[n] = bld128s(rws, OFF_U + (seg * 32 + wid * 4 + g) * 10240u + (cg * 64 + n * 16 + fr) * 16);
            f32x4 c[2][4];
#pragma unroll
            for (int qt = 0; qt < 2; ++qt)
#pragma unroll
                for (int n = 0; n < 4; ++n) {
                    f32x4 t = {0.f, 0.f, 0.f, 0.f};
#pragma unroll
                    for (int hh = 0; hh < 4; ++hh) t = mfma(yb[hh][n], px[hh][qt], t);
                    c[qt][n] = mfma(yu[n], pm[qt], t);
                }
#pragma unroll
            for (int qt = 0; qt < 2; ++qt)
#pragma unroll
                for (int n = 0; n < 4; ++n) *(LDSP f32x4*)(lds + (wid * 8 + qt * 4 + n) * 1024 + lane * 16) = c[qt][n];
            RAW_BARRIER();
            f32x4 s = {0.f, 0.f, 0.f, 0.f};
#pragma unroll
            for (int w = 0; w < 8; ++w) s += *(LDSP f32x4*)(lds + (w * 8 + wid) * 1024 + lane * 16);
            u32x2 o;
            o.x = pack2(blo(xv.x) + bfr(bfr(s[0]) * blo(gv.x)), bhi(xv.x) + bfr(bfr(s[1]) * bhi(gv.x)));
            o.y = pack2(blo(xv.y) + bfr(bfr(s[2]) * blo(gv.y)), bhi(xv.y) + bfr(bfr(s[3]) * bhi(gv.y)));
            *(u32x2*)(a.out + eo) = o;
        }
    }
}

#define BL4 "buffer_load_dwordx4 "
DEV int gather4(u32x4 rs, u32 v0, u32 v1, u32 v2, u32 v3, u32 so, u32x4& t0, u32x4& t1, u32x4& t2, u32x4& t3, bool& dead) {
    asm volatile("s_nop 4\n\t" BL4 "%0, %4, %8, %9 offen sc1\n\t" BL4 "%1, %5, %8, %9 offen sc1\n\t"
                 BL4 "%2, %6, %8, %9 offen sc1\n\t" BL4 "%3, %7, %8, %9 offen sc1\n\ts_waitcnt vmcnt(0)"
                 : "=&v"(t0), "=&v"(t1), "=&v"(t2), "=&v"(t3) : "v"(v0), "v"(v1), "v"(v2), "v"(v3), "s"(rs), "s"(so) : "memory");
    int spins = 0;
    for (;;) {
        const unsigned long long miss = __ballot(umax(umax(max4(t0), max4(t1)), umax(max4(t2), max4(t3))) == SENT);
        if (__builtin_expect(miss == 0, 1) || dead) break;
        asm volatile("s_mov_b64 exec, %10\n\ts_nop 4\n\t" BL4 "%0, %4, %8, %9 offen sc1\n\t" BL4 "%1, %5, %8, %9 offen sc1\n\t"
                     BL4 "%2, %6, %8, %9 offen sc1\n\t" BL4 "%3, %7, %8, %9 offen sc1\n\ts_mov_b64 exec, -1\n\ts_waitcnt vmcnt(0)"
                     : "+v"(t0), "+v"(t1), "+v"(t2), "+v"(t3) : "v"(v0), "v"(v1), "v"(v2), "v"(v3), "s"(rs), "s"(so), "s"(miss) : "memory");
        if (++spins > SPIN_CAP) dead = true;
    }
    return spins;
}
DEV int gather3(u32x4 rs, u32 v0, u32 v1, u32 v2, u32 so, u32x4& t0, u32x4& t1, u32x4& t2, bool& dead) {
    asm volatile("s_nop 4\n\t" BL4 "%0, %3, %6, %7 offen sc1\n\t" BL4 "%1, %4, %6, %7 offen sc1\n\t"
                 BL4 "%2, %5, %6, %7 offen sc1\n\ts_waitcnt vmcnt(0)"
                 : "=&v"(t0), "=&v"(t1), "=&v"(t2) : "v"(v0), "v"(v1), "v"(v2), "s"(rs), "s"(so) : "memory");
    int spins = 0;
    for (;;) {
        const unsigned long long miss = __ballot(umax(umax(max4(t0), max4(t1)), max4(t2)) == SENT);
        if (__builtin_expect(miss == 0, 1) || dead) break;
        asm volatile("s_mov_b64 exec, %8\n\ts_nop 4\n\t" BL4 "%0, %3, %6, %7 offen sc1\n\t" BL4 "%1, %4, %6, %7 offen sc1\n\t"
                     BL4 "%2, %5, %6, %7 offen sc1\n\ts_mov_b64 exec, -1\n\ts_waitcnt vmcnt(0)"
                     : "+v"(t0), "+v"(t1), "+v"(t2) : "v"(v0), "v"(v1), "v"(v2), "s"(rs), "s"(so), "s"(miss) : "memory");
        if (++spins > SPIN_CAP) dead = true;
    }
    return spins;
}
DEV u32 add2(u32 a, u32 b) { return pack2(blo(a) + blo(b), bhi(a) + bhi(b)); }
DEV u32x4 add8(u32x4 a, u32x4 b) { return (u32x4){add2(a.x, b.x), add2(a.y, b.y), add2(a.z, b.z), add2(a.w, b.w)}; }
DEV int memi_unit(int k) { return k ^ (((k >> 4) & 1) << 3); }

constexpr int NPRE = 8;
constexpr int L_MEMI = 0, L_QKI = 10240, L_PI = 16384, L_RING = 32768;

DEV int gather_mem(const Args& a, u32x4 rs, lptr lds, int s, u32 so, u32 tshot, int wid, int lane, int tid, bool& dead) {
    int n = 0;
    if (s == 0) {
        for (int k = tid; k < DM; k += 512) {
            const unsigned short* m = (const unsigned short*)a.mem0 + k;
            u32x4 v;
            v.x = m[0] | ((u32)m[DM] << 16); v.y = m[2 * DM] | ((u32)m[3 * DM] << 16);
            v.z = m[4 * DM] | ((u32)m[5 * DM] << 16); v.w = m[6 * DM] | ((u32)m[7 * DM] << 16);
            *(LDSP u32x4*)(lds + L_MEMI + memi_unit(k) * 16) = v;
        }
        WAIT_V(0);
    } else if (wid >= 3) {
        u32x4 t0, t1, t2, t3;
        lane = opq(lane);
        const u32 o = (wid - 3) * 2048 + lane * 16;
        const u32 v0 = opq(blk(o)), v1 = opq(blk(o + 10240)), v2 = opq(blk(o + 1024)), v3 = opq(blk(o + 11264));
        const int u = opq(memi_unit((wid - 3) * 128 + lane) * 16);
        wait_cyc(tshot);
        n = gather4(rs, v0, v1, v2, v3, so, t0, t1, t2, t3, dead);
        *(LDSP u32x4*)(lds + L_MEMI + u) = add8(t0, t1);
        *(LDSP u32x4*)(lds + L_MEMI + u + 1024) = add8(t2, t3);
    } else WAIT_V(0);
    return n;
}
DEV u32 get_parity(rsrc_t rws, u32* flags, lptr lds, bool prober, int wid, int lane, bool& dead) {
    if (prober) {
        if (wid < 6) {
            u32 best = ~0u;
            for (int k = 0; k < 8; ++k) {
                const u32 c0 = cyc();
                WAIT_L0();
                u32 v = __builtin_amdgcn_raw_buffer_load_b32(rws, OFF_X + (wid >> 1) * 2 * MB + 24u * 81920u + (wid & 1) * 8192u + k * 512u, 0, 16);
                asm volatile("s_waitcnt vmcnt(0)" : "+v"(v)::"memory");
                const u32 d = cyc() - c0;
                best = d < best ? d : best;
            }
            if (lane == 0) ((LDSP u32*)lds)[wid] = best;
        }
        RAW_BARRIER();
        if (wid == 0 && lane == 0) {
            LDSP u32* r = (LDSP u32*)lds;
            const u32 v = 8u | (r[1] < r[0] ? 1u : 0u) | (r[3] < r[2] ? 2u : 0u) | (r[5] < r[4] ? 4u : 0u);
            __hip_atomic_store(flags + F_PAR * 32, v, __ATOMIC_RELAXED, __HIP_MEMORY_SCOPE_AGENT);
        }
        RAW_BARRIER();
    }
    wait_flag(flags, F_PAR, 8, dead);
    return __builtin_amdgcn_readfirstlane(__hip_atomic_load(flags + F_PAR * 32, __ATOMIC_RELAXED, __HIP_MEMORY_SCOPE_AGENT));
}
DEV int a_off(int lane) {
    const int G = lane >> 4, q = (lane >> 2) & 3, p = lane & 3;
    return 16 * memi_unit(16 * G + 4 * (q >> 1) + (q & 1) + 2 * (p >> 1)) + 8 * (p & 1);
}
DEV void stage_rows(u32x4 r, u32 off, int ld, int rowb, int rows, lptr dst, int wid, int lane) {
    const int total = rows * rowb / 1024;
    for (int i = wid; i < total; i += 8) {
        const int b = i * 1024 + lane * 16;
        bdma<0>(r, (u32)(b / rowb) * ld + (b % rowb), off, (u32)(size_t)dst + i * 1024);
    }
}
DEV u32x8 wfrag(lptr img, int rowb, int k0, int col0, int lane) {
    const int g = lane >> 4, q = (lane >> 2) & 3, p = lane & 3;
    lptr b = img + (k0 + 8 * g + q) * rowb + (col0 + 4 * p) * 2;
    return cat(cat(trread(b), trread(b + 4 * rowb)), cat(trread(b + 32 * rowb), trread(b + 36 * rowb)));
}

DEV void chain_s(const Args& a, lptr lds, int h, int wid, int lane, int tid) {
    char* ws = a.ws;
    u32* flags = (u32*)(ws + OFF_FLAGS);
    const rsrc_t rws = __builtin_amdgcn_make_buffer_rsrc(ws, 0, WS_USED, 0x00020000);
    const u32x4 rs = mkrsrc(ws, WS_USED);
    const int g = lane >> 4, fr = lane & 15;
    bool dead = false;
    u32x8 W[2][10];
    {
        const u32x4 rq = mkrsrc(a.Wq, WQ_BYTES), rk = mkrsrc(a.Wk, WQ_BYTES);
        auto st = [&](int pc) {
            lptr d = lds + (pc & 1) * 65536;
            stage_rows(rq, (u32)pc * 128u * 8192u + h * 256, 8192, 256, 128, d, wid, lane);
            stage_rows(rk, (u32)pc * 128u * 8192u + h * 256, 8192, 256, 128, d + 32768, wid, lane);
        };
        st(0);
#pragma unroll
        for (int pc = 0; pc < 5; ++pc) {
            if (pc + 1 < 5) { st(pc + 1); WAIT_V(8); } else WAIT_V(0);
            RAW_BARRIER();
            lptr img = lds + (pc & 1) * 65536 + (wid >> 2) * 32768;
#pragma unroll
            for (int c = 0; c < 2; ++c)
#pragma unroll
                for (int t = 0; t < 2; ++t) W[t][pc * 2 + c] = wfrag(img, 256, c * 64, (wid & 3) * 32 + t * 16, lane);
            RAW_BARRIER();
        }
    }
    const int key0 = 16 * ((fr & 3) >> 1) + 8 * (fr >> 3) + 2 * ((fr >> 2) & 1) + (fr & 1);
    const u32 ldsb = (u32)(size_t)lds;
    auto dma = [&](int seg) {
        const u32 d = ldsb + L_RING + (seg & 1) * 16384;
        const u32 kv = key0 * 8192 + g * 16, qv = fr * 8192 + g * 16;
        const u32 sb = (u32)seg * 32u * 8192u + h * 256;
#pragma unroll
        for (int t = 0; t < 2; ++t)
#pragma unroll
            for (int c = 0; c < 4; ++c) {
                bdma<1>(rs, kv, OFF_K + sb + t * 4 * 8192 + c * 64, d + (t * 4 + c) * 1024);
                bdma<1>(rs, qv, OFF_Q + sb + t * 16 * 8192 + c * 64, d + 8192 + (t * 4 + c) * 1024);
            }
    };
    const u32 par = get_parity(rws, flags, lds, h == 0, wid, lane, dead);
    if (wid == 7) { wait_flag(flags, F_KQ + h, 1, dead); dma(0); dma(1); }
    const int idx = (fr < 8) ? 0x4444 : 0xEEEE;
    const int ao = a_off(lane);
    const float cs = 0.08838834764831845f * 1.4426950408889634f;
    u32 tshot = 0;
    TC_DECL;
    for (int s = 0; s < NSEG; ++s) {
        TC(0);
        const u32 so = xstep(s, par);
        const int nre = gather_mem(a, rs, lds, s, so, tshot, wid, lane, tid, dead);
        TCV(9, nre); TC(1);
        RAW_BARRIER();
        TC(2);
        f32x4 c0 = {0.f, 0.f, 0.f, 0.f}, c1 = c0;
#pragma unroll
        for (int c = 0; c < 10; ++c) {
            const u32x4 av = cat(trread(lds + L_MEMI + c * 1024 + ao), trread(lds + L_MEMI + c * 1024 + (ao ^ 128)));
            c0 = smfmac(av, W[0][c], c0, idx);
            c1 = smfmac(av, W[1][c], c1, idx);
        }
        {
            const u32 d0 = pack2(fold32(c0[0], c1[0]), fold32(c0[1], c1[1])), d1 = pack2(fold32(c0[2], c1[2]), fold32(c0[3], c1[3]));
            LDSP u32* qi = (LDSP u32*)(lds + L_QKI + (wid * 32 + (lane >> 5) * 16 + fr) * 16 + (g & 1) * 4);
            qi[0] = d0; qi[2] = d1;
        }
        RAW_BARRIER();
        TC(3);
        if (wid < 3) {
            lptr rg = lds + L_RING + (s & 1) * 16384;
            lptr qk = lds + L_QKI + (8 * g + ((lane >> 2) & 3)) * 16 + 8 * (lane & 1);
            f32x4 st[3];
#pragma unroll
            for (int t = 0; t < 3; ++t) st[t] = (f32x4){0.f, 0.f, 0.f, 0.f};
#pragma unroll
            for (int c = 0; c < 4; ++c) {
                u32x4 qf;
                if (wid == 0) qf = cat(trread(qk + c * 512), trread(qk + c * 512 + 64));
                else qf = lds128(rg + 8192 + ((wid - 1) * 4 + c) * 1024 + lane * 16);
                const u32x4 km = cat(trread(qk + 2048 + c * 512), trread(qk + 2048 + c * 512 + 64));
                st[0] = mfma(lds128(rg + c * 1024 + lane * 16), qf, st[0]);
                st[1] = mfma(lds128(rg + (4 + c) * 1024 + lane * 16), qf, st[1]);
                st[2] = mfma(km, qf, st[2]);
            }
            const float NEG = -3.0e38f;
            if (wid) {
                const int tq = (wid - 1) * 16 + fr;
#pragma unroll
                for (int t = 0; t < 2; ++t)
#pragma unroll
                    for (int r = 0; r < 4; ++r) {
                        const int key = 16 * (r >> 1) + 8 * (g >> 1) + 4 * t + 2 * (g & 1) + (r & 1);
                        st[t][r] = key <= tq ? st[t][r] : NEG;
                    }
            }
            float m = fmaxf(fmaxf(fmaxf(st[0][0], st[0][1]), fmaxf(st[0][2], st[0][3])), fmaxf(fmaxf(st[1][0], st[1][1]), fmaxf(st[1][2], st[1][3])));
            m = fmaxf(m, fmaxf(fmaxf(st[2][0], st[2][1]), fmaxf(st[2][2], st[2][3])));
            m = xmax(m);
            const float mc = m * cs;
            float sum = 0.f;
#pragma unroll
            for (int t = 0; t < 3; ++t)
#pragma unroll
                for (int r = 0; r < 4; ++r) {
                    const float e = __builtin_amdgcn_exp2f(__builtin_fmaf(st[t][r], cs, -mc));
                    st[t][r] = e;
                    sum += (t == 2 && g >= 2) ? 0.f : e;
                }
            sum = xsum(sum);
            const float inv = 1.0f / sum;
            u32 d[3][2];
#pragma unroll
            for (int t = 0; t < 3; ++t) { d[t][0] = pack2(st[t][0] * inv, st[t][1] * inv); d[t][1] = pack2(st[t][2] * inv, st[t][3] * inv); }
            if (wid == 0) {
                if (s + 1 < NSEG) {
                    const int qi = 4 * ((fr >> 1) & 1) + 2 * ((fr >> 2) & 1) + (fr & 1);
                    u32 e0 = d[2][0], e1 = d[2][1], f0 = e0, f1 = e1;
                    swap16(e0, f0); swap16(e1, f1);
                    const bool xp = fr < 8;
                    const u32x4 v = {nosent(xp ? d[0][0] : e0), nosent(xp ? d[0][1] : e1), nosent(xp ? d[1][0] : f0), nosent(xp ? d[1][1] : f1)};
                    if (xp || g == 0) __builtin_amdgcn_raw_buffer_store_b128(v, rws, blk(20480u + h * 640u + (xp ? (8 * g + qi) * 16 : 512 + qi * 16)), so, 16);
                    TC(4);
                }
            } else {
                swap16(d[0][0], d[1][0]); swap16(d[0][1], d[1][1]);
                bst128<16>(rws, (u32x4){d[0][0], d[1][0], d[0][1], d[1][1]}, OFF_PX + (u32)(s * 32 + h) * 2048u + (wid - 1) * 1024 + lane * 16);
                u32 e0 = d[2][0], e1 = d[2][1];
                swap16(d[2][0], e0); swap16(d[2][1], e1);
                if (g == 0) bst128<16>(rws, (u32x4){d[2][0], e0, d[2][1], e1}, OFF_PXM + (u32)(s * 8 + (h >> 2)) * 2048u + (wid - 1) * 1024 + ((h & 3) * 16 + fr) * 16);
                if (s >= NSEG - 2) {
                    WAIT_V(0);
                    if (lane == 0) flag_add(flags, F_X + s);
                }
            }
        } else tshot = cyc() + a.ds;
        if (wid == 3 && h == 0 && s >= 2 && lane == 0) __hip_atomic_store(flags + (F_X + s - 2) * 32, 96u, __ATOMIC_RELAXED, __HIP_MEMORY_SCOPE_AGENT);
        if (wid == 7 && s >= 1 && s + 1 < NSEG) {
            if (((s + 1) & 3) == 0) wait_flag(flags, F_KQ + ((s + 1) >> 2) * 32 + h, 1, dead);
            dma(s + 1);
        }
        TC_FLUSH(s);
    }
    TC_DUMP();
}

DEV void chain_v(const Args& a, lptr lds, int h, int wid, int lane, int tid) {
    char* ws = a.ws;
    u32* flags = (u32*)(ws + OFF_FLAGS);
    const rsrc_t rws = __builtin_amdgcn_make_buffer_rsrc(ws, 0, WS_USED, 0x00020000);
    const u32x4 rs = mkrsrc(ws, WS_USED);
    const int g = lane >> 4, fr = lane & 15;
    bool dead = false;
    u32x8 Wv[10], Wo[5][2];
    {
        const u32x4 rv = mkrsrc(a.Wv, WQ_BYTES), ro = mkrsrc(a.Wo, WQ_BYTES);
        auto stv = [&](int pc) { stage_rows(rv, (u32)pc * 320u * 8192u + h * 256, 8192, 256, 320, lds + (pc & 1) * 81920, wid, lane); };
        auto sto = [&](int pc) { stage_rows(ro, (u32)(h * 128 + pc * 64) * 1280u, 1280, 1280, 64, lds + (pc & 1) * 81920, wid, lane); };
        stv(0); stv(1);
#pragma unroll
        for (int pc = 0; pc < 2; ++pc) {
            WAIT_V(10);
            RAW_BARRIER();
#pragma unroll
            for (int c = 0; c < 5; ++c) Wv[pc * 5 + c] = wfrag(lds + pc * 81920, 256, c * 64, wid * 16, lane);
            RAW_BARRIER();
            sto(pc);
        }
#pragma unroll
        for (int pc = 0; pc < 2; ++pc) {
            if (pc == 0) WAIT_V(10); else WAIT_V(0);
            RAW_BARRIER();
#pragma unroll
            for (int t = 0; t < 5; ++t) Wo[t][pc] = wfrag(lds + pc * 81920, 1280, 0, wid * 80 + t * 16, lane);
        }
        RAW_BARRIER();
    }
    const int grp = h >> 4, j = h & 15;
    const int ntile = j < 8 ? 3 : 2, tile0 = j < 8 ? 3 * j : 24 + 2 * (j - 8);
    const u32 ldsb = (u32)(size_t)lds;
    auto dma = [&](int seg) {
        const u32 d = ldsb + L_RING + (seg & 1) * 49152 + wid * 2048;
        const u32 sb = OFF_B + ((u32)(seg * 32 + grp * 16 + wid * 2) * 40u + tile0) * 1024u;
#pragma unroll
        for (int t = 0; t < 3; ++t)
            if (t < ntile) {
                bdma<1>(rs, lane * 16, sb + t * 1024, d + t * 16384);
                bdma<1>(rs, lane * 16, sb + (40 + t) * 1024, d + t * 16384 + 1024);
            }
    };
    const u32 par = get_parity(rws, flags, lds, false, wid, lane, dead);
    wait_flag(flags, F_VB + grp, 16, dead);
    dma(0);
    WAIT_V(0);
    u32 tshot = 0;
    TC_DECL;
    const int idx = (fr < 8) ? 0x4444 : 0xEEEE;
    const int ao = a_off(lane);
    for (int s = 0; s < NSEG; ++s) {
        TC(0);
        const u32 so = xstep(s, par);
        const int nre = gather_mem(a, rs, lds, s, so, tshot, wid, lane, tid, dead);
        TCV(9, nre); TC(1);
        RAW_BARRIER();
        TC(2);
        {
            f32x4 c0 = {0.f, 0.f, 0.f, 0.f};
#pragma unroll
            for (int c = 0; c < 10; ++c) {
                const u32x4 av = cat(trread(lds + L_MEMI + c * 1024 + ao), trread(lds + L_MEMI + c * 1024 + (ao ^ 128)));
                c0 = smfmac(av, Wv[c], c0, idx);
            }
            if (s + 1 < NSEG - 1) dma(s + 1);
            const u32 d = pack2(fold32(c0[0], c0[2]), fold32(c0[1], c0[3]));
            *(LDSP u32*)(lds + L_QKI + memi_unit(wid * 16 + fr) * 16 + (2 * (g & 1) + (g >> 1)) * 4) = d;
        }
        RAW_BARRIER();
        TC(3);
        {
            f32x4 u[5];
#pragma unroll
            for (int t = 0; t < 5; ++t) u[t] = (f32x4){0.f, 0.f, 0.f, 0.f};
#pragma unroll
            for (int c = 0; c < 2; ++c) {
                const u32x4 av = cat(trread(lds + L_QKI + c * 1024 + ao), trread(lds + L_QKI + c * 1024 + (ao ^ 128)));
#pragma unroll
                for (int t = 0; t < 5; ++t) u[t] = smfmac(av, Wo[t][c], u[t], idx);
            }
            u32 x0 = pack2(fold32(u[0][0], u[1][0]), fold32(u[0][1], u[1][1])), x1 = pack2(fold32(u[0][2], u[1][2]), fold32(u[0][3], u[1][3]));
            u32 y0 = pack2(fold32(u[2][0], u[3][0]), fold32(u[2][1], u[3][1])), y1 = pack2(fold32(u[2][2], u[3][2]), fold32(u[2][3], u[3][3]));
            swap16(x0, y0); swap16(x1, y1);
            const u32 ub = OFF_U + (u32)(s * 32 + h) * 10240u + wid * 1280;
            bst128<16>(rws, (u32x4){nosent(x0), nosent(x1), nosent(y0), nosent(y1)}, ub + (((g & 1) * 2 + (g >> 1)) * 16 + fr) * 16);
            u32 e0 = pack2(fold32(u[4][0], u[4][2]), fold32(u[4][1], u[4][3])), e1 = e0;
            swap32(e0, e1);
            u32 f0 = e0, f1 = e1;
            swap16(e0, f0); swap16(e1, f1);
            if (g == 0) bst128<16>(rws, (u32x4){nosent(e0), nosent(e1), nosent(f0), nosent(f1)}, ub + (64 + fr) * 16);
        }
        TC(4);
        if (s + 1 < NSEG) {
            const u32 tref = cyc();
            if (wid < ntile) {
                u32x4 yb[NPRE], yu0, yu1, yu2, yu3;
                lptr rg = lds + L_RING + (s & 1) * 49152 + wid * 16384 + lane * 16;
#pragma unroll
                for (int hh = 0; hh < NPRE; ++hh) yb[hh] = lds128(rg + hh * 1024);
                const int ln = opq(lane);
                const u32 vo = (u32)(ln >> 4) * 10240u + ((tile0 + wid) * 16 + (ln & 15)) * 16;
                wait_cyc(tref + a.du);
                const int nru = gather4(rs, vo, vo + 4 * 10240, vo + 8 * 10240, vo + 12 * 10240, OFF_U + (u32)(s * 32 + grp * 16) * 10240u, yu0, yu1, yu2, yu3, dead);
                TCV(5, nru);
                TC(6);
                RAW_BARRIER();
                TC(7);
                f32x4 c0 = {0.f, 0.f, 0.f, 0.f};
                lptr px = lds + L_PI + (lane >> 5) * 640 + (lane & 31) * 16;
#pragma unroll
                for (int c = 0; c < NPRE / 2; ++c) c0 = smfmac(lds128(px + c * 1280), cat(yb[2 * c], yb[2 * c + 1]), c0, idx);
#pragma unroll
                for (int c = 0; c < 2; ++c) {
                    lptr pm = lds + L_PI + 512 + c * 5120 + g * 1280 + (fr & 7) * 16 + (fr >> 3) * 8;
                    c0 = smfmac(cat(*(LDSP u32x2*)pm, *(LDSP u32x2*)(pm + 640)), c ? cat(yu2, yu3) : cat(yu0, yu1), c0, idx);
                }
#pragma unroll
                for (int c = NPRE / 2; c < 8; ++c)
                    c0 = smfmac(lds128(px + c * 1280), cat(lds128(rg + 2 * c * 1024), lds128(rg + 2 * c * 1024 + 1024)), c0, idx);
                u32 e0 = pack2(fold32(c0[0], c0[2]), fold32(c0[1], c0[3])), e1 = e0;
                swap32(e0, e1);
                u32 f0 = e0, f1 = e1;
                swap16(e0, f0); swap16(e1, f1);
                if (g == 0) __builtin_amdgcn_raw_buffer_store_b128((u32x4){nosent(e0), nosent(e1), nosent(f0), nosent(f1)}, rws,
                                                                   blk(grp * 10240u + ((tile0 + wid) * 16 + fr) * 16), xstep(s + 1, par), 16);
                TC(8);
            } else {
                if (wid >= 3 && wid < 7) {
                    const int c0 = wid < 5 ? (wid - 3) * 3 : 6 + (wid - 5) * 2, c2 = wid < 5 ? c0 + 2 : c0 + 1;
                    const int ln = opq(lane);
                    const u32 po = 20480u + grp * 10240u + ln * 16;
                    const u32 v0 = opq(blk(po + c0 * 1024)), v1 = opq(blk(po + c0 * 1024 + 1024)), v2 = opq(blk(po + c2 * 1024));
                    u32x4 t0, t1, t2;
                    wait_cyc(tref + a.dp);
                    const int nrp = gather3(rs, v0, v1, v2, so, t0, t1, t2, dead);
                    TCV(5, nrp);
                    *(LDSP u32x4*)(lds + L_PI + c0 * 1024 + ln * 16) = t0;
                    *(LDSP u32x4*)(lds + L_PI + c0 * 1024 + 1024 + ln * 16) = t1;
                    *(LDSP u32x4*)(lds + L_PI + c2 * 1024 + ln * 16) = t2;
                    TC(6);
                } else {
                    if (wid == 7 && ((s + 2) & 3) == 0 && s + 2 < NSEG - 1) wait_flag(flags, F_VB + ((s + 2) >> 2) * 2 + grp, 16, dead);
                    WAIT_V(0);
                }
                RAW_BARRIER();
                tshot = cyc() + a.dv;
                if (s == NSEG - 2 && tid == 448) flag_add(flags, F_X + s);
            }
        }
        TC_FLUSH(s);
    }
    TC_DUMP();
    WAIT_V(0);
    RAW_BARRIER();
    if (tid == 0) flag_add(flags, F_X + NSEG - 1);
}

__global__ void __launch_bounds__(512, 2) main_kernel(Args a) {
    __shared__ __attribute__((aligned(1024))) char smem[163840];
    lptr lds = (lptr)smem;
    const int tid = threadIdx.x, lane = tid & 63, wid = __builtin_amdgcn_readfirstlane(tid >> 6);
    a.ws = ws_base(a.ws);
    const int x = blockIdx.x & 7, t = blockIdx.x >> 3;
    if (x >= 2) worker(a, lds, (LDSP int*)(lds + 163824), wid, lane, tid);
    else if (t < 16) chain_s(a, lds, x * 16 + t, wid, lane, tid);
    else chain_v(a, lds, x * 16 + t - 16, wid, lane, tid);
}

extern "C" void kernel_launch(void* const* d_in, const int* in_sizes, int n_in, void* d_out, int out_size, void* d_ws, size_t ws_size,
                              hipStream_t stream) {
    Args a;
    a.x = (const char*)d_in[0]; a.mem0 = (const char*)d_in[1]; a.Wq = (const char*)d_in[2]; a.Wk = (const char*)d_in[3];
    a.Wv = (const char*)d_in[4]; a.Wo = (const char*)d_in[5]; a.w1 = (const char*)d_in[8]; a.b1 = (const char*)d_in[9];
    a.w2 = (const char*)d_in[10]; a.b2 = (const char*)d_in[11];
    a.out = (char*)d_out; a.ws = (char*)d_ws;
    a.ds = 3600; a.dv = 1100; a.dp = 1500; a.du = 500;
    prologue_kernel<<<512, 256, 0, stream>>>((const u32*)d_in[0], (char*)d_ws);
    main_kernel<<<256, 512, 0, stream>>>(a);
}
```

```cpp
#include <hip/hip_runtime.h>
#include <hip/hip_bf16.h>
#include <stdint.h>
#pragma clang diagnostic ignored "-Winline-asm"

typedef unsigned u32;
typedef u32 u32x2 __attribute__((ext_vector_type(2)));
typedef u32 u32x4 __attribute__((ext_vector_type(4)));
typedef u32 u32x8 __attribute__((ext_vector_type(8)));
typedef float f32x4 __attribute__((ext_vector_type(4)));
typedef float f32x2 __attribute__((ext_vector_type(2)));
typedef __bf16 bf16x2 __attribute__((ext_vector_type(2)));
typedef __bf16 bf16x8 __attribute__((ext_vector_type(8)));
typedef __bf16 bf16x16 __attribute__((ext_vector_type(16)));
typedef short s16x4 __attribute__((ext_vector_type(4)));
#define LDSP __attribute__((address_space(3)))
typedef LDSP char* lptr;
#define DEV __device__ __forceinline__

constexpr int SEQ = 2048, DM = 640, NH = 32, DH = 128, NSEG = 64, FF = 1728;
constexpr u32 SENT = 0xFFFFFFFFu;
constexpr u32 WQ_BYTES = 640u * 4096u * 2u, W1_BYTES = 640u * 1728u * 2u;
constexpr int SPIN_CAP = 400000;

constexpr u32 MB = 1u << 20;
constexpr u32 OFF_FLAGS = 0;
constexpr u32 FLAG_BYTES = 32 * 1024;
constexpr u32 OFF_X = 2 * MB, XP_PX = 12288, XP_PM = 28672, XP_U = 32768, XP_W = 44 * 8192;
constexpr u32 OFF_U = 6 * MB;
constexpr u32 OFF_XN = 32 * MB;
constexpr u32 OFF_G = 36 * MB;
constexpr u32 OFF_H = 40 * MB;
constexpr u32 OFF_PX = 48 * MB;
constexpr u32 OFF_PXM = 54 * MB;
constexpr u32 OFF_Q = 56 * MB;
constexpr u32 OFF_K = 76 * MB;
constexpr u32 OFF_B = 96 * MB;
constexpr u32 WS_USED = 176 * MB;
constexpr int F_QO = 7, F_QG = 8, F_PAR = 9, F_QE = 6, F_KQ = 16  , F_VB = 18  , F_H = 48  , F_G = 64  , F_S = 80  , F_X = 96  ;
constexpr u32 FX_FULL = 96;

#define TC_DECL do {} while (0)
#define TC(k) do {} while (0)
#define TCV(k, v) do {} while (0)
#define TC_FLUSH(s) do {} while (0)
#define TC_DUMP() do {} while (0)
#define TS(slot, k) do {} while (0)
#define TSV(slot, k, v) do {} while (0)
#define WAIT_V(n) do { asm volatile("" ::: "memory"); __builtin_amdgcn_s_waitcnt(0xF70 | ((n) & 15) | (((n) >> 4) << 14)); asm volatile("" ::: "memory"); } while (0)
#define WAIT_L0() do { asm volatile("" ::: "memory"); __builtin_amdgcn_s_waitcnt(0xC07F); asm volatile("" ::: "memory"); } while (0)
#define SCHED() __builtin_amdgcn_sched_barrier(0)
#define RAW_BARRIER() do { WAIT_L0(); __builtin_amdgcn_s_barrier(); asm volatile("" ::: "memory"); } while (0)

DEV u32 pack2(float a, float b) { bf16x2 v = {(__bf16)a, (__bf16)b}; return __builtin_bit_cast(u32, v); }
DEV float bfr(float a) { return (float)(__bf16)a; }
DEV float blo(u32 d) { return __builtin_bit_cast(float, d << 16); }
DEV float bhi(u32 d) { return __builtin_bit_cast(float, d & 0xffff0000u); }
DEV u32 fbits(float f) { return __builtin_bit_cast(u32, f); }
DEV float bitsf(u32 u) { return __builtin_bit_cast(float, u); }
DEV u32 nosent(u32 d) { return d < 0xFFFFFFFEu ? d : 0xFFFFFFFEu; }
DEV void swap32(u32& a, u32& b) { auto r = __builtin_amdgcn_permlane32_swap(a, b, false, false); a = r[0]; b = r[1]; }
DEV void swap16(u32& a, u32& b) { auto r = __builtin_amdgcn_permlane16_swap(a, b, false, false); a = r[0]; b = r[1]; }
DEV float fold32(float a, float b) { u32 x = fbits(a), y = fbits(b); swap32(x, y); return bitsf(x) + bitsf(y); }
DEV float xmax(float v) {
    u32 a = fbits(v), b = a; swap32(a, b); v = fmaxf(bitsf(a), bitsf(b));
    a = fbits(v); b = a; swap16(a, b); return fmaxf(bitsf(a), bitsf(b));
}
DEV float xsum(float v) {
    u32 a = fbits(v), b = a; swap32(a, b); v = bitsf(a) + bitsf(b);
    a = fbits(v); b = a; swap16(a, b); return bitsf(a) + bitsf(b);
}
DEV u32x2 trread(lptr p) { s16x4 v = __builtin_amdgcn_ds_read_tr16_b64_v4i16((LDSP s16x4*)p); return __builtin_bit_cast(u32x2, v); }
DEV u32x4 lds128(lptr p) { return *(LDSP u32x4*)p; }
DEV u32x4 cat(u32x2 a, u32x2 b) { return (u32x4){a.x, a.y, b.x, b.y}; }
DEV u32x8 cat(u32x4 a, u32x4 b) { return (u32x8){a.x, a.y, a.z, a.w, b.x, b.y, b.z, b.w}; }
DEV f32x4 mfma(u32x4 a, u32x4 b, f32x4 c) {
    return __builtin_amdgcn_mfma_f32_16x16x32_bf16(__builtin_bit_cast(bf16x8, a), __builtin_bit_cast(bf16x8, b), c, 0, 0, 0);
}
DEV f32x4 smfmac(u32x4 a, u32x8 b, f32x4 c, int idx) {
    return __builtin_amdgcn_smfmac_f32_16x16x64_bf16(__builtin_bit_cast(bf16x8, a), __builtin_bit_cast(bf16x16, b), c, idx, 0, 0);
}
template <int SC1> DEV void bdma(u32x4 rs, u32 vo, u32 so, u32 lo) {
    if (SC1) asm volatile("s_mov_b32 m0, %3\n\ts_nop 3\n\tbuffer_load_dwordx4 %0, %1, %2 offen sc1 lds" ::"v"(vo), "s"(rs), "s"(so), "s"(lo) : "memory", "m0");
    else asm volatile("s_mov_b32 m0, %3\n\ts_nop 3\n\tbuffer_load_dwordx4 %0, %1, %2 offen lds" ::"v"(vo), "s"(rs), "s"(so), "s"(lo) : "memory", "m0");
}
DEV u32x4 mkrsrc(const void* p, u32 bytes) {
    unsigned long long a = (unsigned long long)p;
    u32x4 r = {(u32)a, (u32)(a >> 32) & 0xffffu, bytes, 0x00020000u};
    r.x = __builtin_amdgcn_readfirstlane(r.x); r.y = __builtin_amdgcn_readfirstlane(r.y);
    r.z = __builtin_amdgcn_readfirstlane(r.z); r.w = __builtin_amdgcn_readfirstlane(r.w);
    return r;
}
DEV char* ws_base(char* ws) { return ws + ((0u - (u32)(unsigned long long)ws) & (2 * MB - 1)); }
DEV u32 blk(u32 o) { return (o >> 13) * 16384u + (o & 8191u); }
DEV u32 xstep(int s, u32 par) { return OFF_X + (u32)(s & 3) * MB + ((par >> ((s >> 1) & 1)) & 1u) * 8192u; }
DEV int opq(int v) { asm volatile("" : "+v"(v)); return v; }
DEV u32 cyc() { return (u32)__builtin_amdgcn_s_memtime(); }
DEV u32 now4() { return (u32)__builtin_amdgcn_s_memrealtime() * 4u; }
DEV void wait_slot(u32 t) {
    int rem;
    for (;;) {
        rem = (int)(t - now4());
        if (rem <= 32 || rem > 8000) break;
        if (rem > 100) __builtin_amdgcn_s_sleep(1);
    }
#pragma unroll
    for (int i = 0; i < 5; ++i)
        if (rem >= 19 + 3 * i && rem <= 32) asm volatile("s_nop 11");
}
DEV u32 max4(u32x4 v) { u32 a = v.x > v.y ? v.x : v.y, b = v.z > v.w ? v.z : v.w; return a > b ? a : b; }
DEV u32 umax(u32 a, u32 b) { return a > b ? a : b; }

typedef __amdgpu_buffer_rsrc_t rsrc_t;
template <int AUX> DEV void bst128(rsrc_t r, u32x4 v, u32 off) { __builtin_amdgcn_raw_buffer_store_b128(v, r, off, 0, AUX); }
template <int AUX> DEV void bst64(rsrc_t r, u32x2 v, u32 off) { __builtin_amdgcn_raw_buffer_store_b64(v, r, off, 0, AUX); }
template <int AUX> DEV void bst32(rsrc_t r, u32 v, u32 off) { __builtin_amdgcn_raw_buffer_store_b32(v, r, off, 0, AUX); }
DEV u32x4 bld128s(rsrc_t r, u32 off) { return __builtin_amdgcn_raw_buffer_load_b128(r, off, 0, 16); }

DEV void wait_flag(u32* flags, int idx, u32 target, bool& dead) {
    if (dead) return;
    int spins = 0;
    for (;;) {
        u32 v = __hip_atomic_load(flags + idx * 32, __ATOMIC_RELAXED, __HIP_MEMORY_SCOPE_AGENT);
        if (__builtin_amdgcn_readfirstlane(v) >= target) break;
        if (++spins > SPIN_CAP) { dead = true; break; }
        __builtin_amdgcn_s_sleep(8);
    }
    asm volatile("" ::: "memory");
}
DEV u32 prog_load(rsrc_t rws, int line, int h0, int n, int lane) {
    return 2u * __builtin_amdgcn_raw_buffer_load_b32(rws, OFF_FLAGS + (line + (lane >> 5)) * 128 + (h0 + (lane & (n - 1))) * 4, 0, 16) + (lane >> 5);
}
DEV void prog_eval(u32 v, int seg, int& known) {
    if (__ballot(v > (u32)seg) == ~0ull) known = __ballot(v > (u32)seg + 8) == ~0ull ? seg + 9 : seg + 1;
}
DEV void wait_prog(rsrc_t rws, int line, int h0, int n, int seg, int& known, int lane, bool& dead, int nap = 1) {
    for (int spins = 0; known <= seg && !dead; ++spins) {
        asm volatile("" ::: "memory");
        prog_eval(prog_load(rws, line, h0, n, lane), seg, known);
        if (known > seg) break;
        if (spins > SPIN_CAP) dead = true;
        for (int i = 0; i < nap; ++i) __builtin_amdgcn_s_sleep(4);
    }
    asm volatile("" ::: "memory");
}
DEV void flag_add(u32* flags, int idx, u32 n = 1u) { __hip_atomic_fetch_add(flags + idx * 32, n, __ATOMIC_RELAXED, __HIP_MEMORY_SCOPE_AGENT); }

__global__ void __launch_bounds__(256) prologue_kernel(const u32* __restrict__ x, char* __restrict__ ws0) {
    char* ws = ws_base(ws0);
    const int tid = threadIdx.x, lane = tid & 63, wid = tid >> 6;
    const int row = blockIdx.x * 4 + wid;
    u32 d[5];
#pragma unroll
    for (int i = 0; i < 5; ++i) d[i] = x[row * 320 + i * 64 + lane];
    {
        const u32x4 s = {SENT, SENT, SENT, SENT};
        const u32x4 z = {0, 0, 0, 0};
        const u32 j = blockIdx.x * 256 + tid, bi = j >> 10;
        if (bi < 96) ((u32x4*)(ws + OFF_X + (bi < 50 ? bi * 16384u : bi < 94 ? MB + (bi - 50) * 16384u : 2 * MB + (bi - 94) * 16384u)))[j & 1023] = s;
        if (j >= 65536) ((u32x4*)(ws + (j < 98304 ? OFF_Q : OFF_K)))[j & 32767] = s;
        if (j < FLAG_BYTES / 16) ((u32x4*)(ws + OFF_FLAGS))[j] = z;
    }
    float v[10], s = 0.f;
#pragma unroll
    for (int i = 0; i < 5; ++i) { v[2 * i] = blo(d[i]); v[2 * i + 1] = bhi(d[i]); s += v[2 * i] + v[2 * i + 1]; }
#pragma unroll
    for (int o = 32; o; o >>= 1) s += __shfl_xor(s, o);
    const float mean = s * (1.0f / DM);
    float q = 0.f;
#pragma unroll
    for (int i = 0; i < 10; ++i) { float t = v[i] - mean; q += t * t; }
#pragma unroll
    for (int o = 32; o; o >>= 1) q += __shfl_xor(q, o);
    const float mb = bfr(mean), vb = bfr(q * (1.0f / DM));
    const float den = bfr(sqrtf(bfr(vb + 1e-5f)));
    u32* xn = (u32*)(ws + OFF_XN);
#pragma unroll
    for (int i = 0; i < 5; ++i) xn[row * 320 + i * 64 + lane] = pack2(bfr(v[2 * i] - mb) / den, bfr(v[2 * i + 1] - mb) / den);
}

template <int NT, int XDEV>
DEV void gemm_core(f32x4 (&acc)[4][NT], u32x4 rX, u32 xoff, int ldx, u32x4 rW, u32 woff, int ldw, int chmax1, int nkt, lptr lds, int wid, int lane) {
    constexpr int NP = NT / 2, STAGE = 16384 * (1 + NP), G = 2 + 2 * NP, D = NT == 2 ? 3 : 2;
    const int sb = lane * 16, swz = sb ^ (((sb >> 9) & 1) << 5);
    const u32 xv = (u32)(16 * wid + (swz >> 6)) * ldx + (swz & 63);
    u32 wv0[2], wv1[2];
#pragma unroll
    for (int i = 0; i < 2; ++i) {
        const int row = 8 * wid + 4 * i + (lane >> 4);
        const int ch = (lane & 15) ^ (((row & 3) << 2) | ((row >> 2) & 3));
        wv0[i] = (u32)row * ldw + ch * 16;
        wv1[i] = (u32)row * ldw + (ch < chmax1 ? ch : chmax1) * 16;
    }
    const u32 ldsb = (u32)(size_t)lds;
    auto piece = [&](int kt, int buf, int j) {
        const u32 b = ldsb + buf * STAGE, wk = woff + (u32)kt * 64u * ldw;
        if (j < 2) bdma<XDEV>(rX, xv, xoff + kt * 128 + j * 64, b + wid * 2048 + j * 1024);
        else if (j < 4) bdma<0>(rW, wv0[j & 1], wk, b + 16384 + (8 * wid + 4 * (j & 1)) * 256);
        else if (j < G) bdma<0>(rW, wv1[j & 1], wk + 256, b + 32768 + (8 * wid + 4 * (j & 1)) * 256);
    };
    const int wr = wid >> 2, wc = wid & 3, g = lane >> 4, q = (lane >> 2) & 3, p = lane & 3;
    const int ob = (lane & 15) * 64 + g * 16;
    const int xo = wr * 8192 + (ob ^ (((ob >> 9) & 1) << 5));
    int wo[NT][2];
#pragma unroll
    for (int n = 0; n < NT; ++n)
#pragma unroll
        for (int t = 0; t < 2; ++t) {
            const int panel = (NT == 4) ? (wc >> 1) : 0;
            const int c = (NT == 4) ? ((wc & 1) * 4 + n) : (wc * 2 + n);
            wo[n][t] = 16384 * (1 + panel) + 256 * (8 * g + 4 * t + q) + 16 * ((2 * c + (p >> 1)) ^ ((q << 2) | ((2 * g + t) & 3))) + 8 * (p & 1);
        }
#pragma unroll
    for (int m = 0; m < 4; ++m)
#pragma unroll
        for (int n = 0; n < NT; ++n) acc[m][n] = (f32x4){0.f, 0.f, 0.f, 0.f};
#pragma unroll
    for (int j = 0; j < D * G; ++j) piece(j / G, j / G, j % G);
    int buf = 0;
    u32x4 xa[4], wa[NT], xb[4], wb[NT];
#pragma unroll
    for (int m = 0; m < 4; ++m) xb[m] = (u32x4){0, 0, 0, 0};
#pragma unroll
    for (int n = 0; n < NT; ++n) wb[n] = (u32x4){0, 0, 0, 0};
    auto mm = [&](const u32x4 (&xf)[4], const u32x4 (&wf)[NT], int kt, int j0) {
#pragma unroll
        for (int m = 0; m < 4; ++m) {
#pragma unroll
            for (int n = 0; n < NT; ++n) acc[m][n] = mfma(wf[n], xf[m], acc[m][n]);
            SCHED();
            if (j0 + m < G && kt < nkt) piece(kt, buf >= 1 ? buf - 1 : D, j0 + m);
            SCHED();
        }
    };
#pragma unroll 1
    for (int kt = 0; kt < nkt; ++kt) {
        if (kt + D - 1 < nkt) WAIT_V((D - 1) * G); else if (kt + 1 < nkt) WAIT_V(G); else WAIT_V(0);
        RAW_BARRIER();
        lptr b = lds + buf * STAGE;
#pragma unroll
        for (int n = 0; n < NT; ++n) wa[n] = cat(trread(b + wo[n][0]), trread(b + wo[n][1]));
#pragma unroll
        for (int m = 0; m < 4; ++m) xa[m] = lds128(b + xo + m * 2048);
        SCHED();
        mm(xb, wb, kt + D, 0);
        SCHED();
#pragma unroll
        for (int n = 0; n < NT; ++n) wb[n] = cat(trread(b + wo[n][0] + 8192), trread(b + wo[n][1] + 8192));
#pragma unroll
        for (int m = 0; m < 4; ++m) xb[m] = lds128(b + xo + m * 2048 + 1024);
        SCHED();
        mm(xa, wa, kt + D, 4);
        SCHED();
        buf = (buf == D) ? 0 : buf + 1;
    }
    mm(xb, wb, 0, G);
}
DEV u32x4 pair8(u32 a0, u32 a1, u32 b0, u32 b1) { swap16(a0, b0); swap16(a1, b1); return (u32x4){a0, a1, b0, b1}; }

struct Args {
    const char *x, *mem0, *Wq, *Wk, *Wv, *Wo, *w1, *b1, *w2, *b2;
    char *out, *ws;
    int T, fv, fp, fu, fw, l1, r0, r1;
};

DEV void worker(const Args& a, lptr lds, LDSP int* bc, int nit, int wid, int lane, int tid) {
    char* ws = a.ws;
    u32* flags = (u32*)(ws + OFF_FLAGS);
    const rsrc_t rws = __builtin_amdgcn_make_buffer_rsrc(ws, 0, WS_USED, 0x00020000);
    const u32x4 rs = mkrsrc(ws, WS_USED);
    const int wr = wid >> 2, wc = wid & 3, g = lane >> 4, fr = lane & 15;
    const int c8 = 8 * ((g >> 1) + 2 * (g & 1));
    const int nc = (a.fv >> 8) & 15, per = 10 / nc, sw = (a.fv >> 12) & 255, ge = a.fv >> 20;
    const bool ow = (a.r1 >> ((blockIdx.x >> 3) & 7)) & 1;
    bool dead = false;
    int known = 0, q = ow ? 0 : 1;
    for (;;) {
        if (wid == 0 && nit < 80) TS(nit, 0);
        if (tid == 0) {
            int type = -1, p0 = 0, p1 = 0;
            if (q == 0) {
                const int it = (int)__hip_atomic_fetch_add(flags + F_QE * 32, 1u, __ATOMIC_RELAXED, __HIP_MEMORY_SCOPE_AGENT);
                if (it < ge * 4 * per) { type = 4; p0 = it / per; p1 = (it % per) * nc + nc * 16; }
                else q = (a.fv & 32) ? 2 : 1;
            }
            if (q == 1) {
                const int it = (int)__hip_atomic_fetch_add(flags + F_QG * 32, 1u, __ATOMIC_RELAXED, __HIP_MEMORY_SCOPE_AGENT);
                if (it < 112) { type = 2; p0 = (it / 7 + ge) & 15; p1 = it % 7; }
                else if (it < 192) { type = 3; p0 = ((it - 112) / 5 + ge) & 15; p1 = (it - 112) % 5; }
                else q = 2;
            }
            if (q == 2 && ow) {
                const int it = (int)__hip_atomic_fetch_add(flags + F_QO * 32, 1u, __ATOMIC_RELAXED, __HIP_MEMORY_SCOPE_AGENT) + ge * 4 * per;
                if (it < sw * per) { type = 4; p0 = it / per; p1 = (it % per) * nc + nc * 16; }
                else if (it - sw * per < (NSEG - sw) * 10) { type = 4; p0 = sw + (it - sw * per) / 10; p1 = (it - sw * per) % 10 + 16; }
            }
            bc[0] = type; bc[1] = p0; bc[2] = p1;
        }
        RAW_BARRIER();
        const int type = __builtin_amdgcn_readfirstlane(bc[0]), p0 = __builtin_amdgcn_readfirstlane(bc[1]), p1 = __builtin_amdgcn_readfirstlane(bc[2]);
        RAW_BARRIER();
        if (type < 0) break;
        if (wid == 0 && nit < 80) { TS(nit, 1); TSV(nit, 2, type * 65536 + p0 * 256 + p1 + 1); }
        ++nit;
        if (type == 2) {
            f32x4 acc[4][4];
            {
                const u32x4 r0 = mkrsrc(a.w1, W1_BYTES);
                gemm_core<4, 0>(acc, rs, OFF_XN + (u32)p0 * 128u * 1280u, 1280, r0, p1 * 512, FF * 2, p1 == 6 ? 7 : 15, 10, lds, wid, lane);
            }
            {
                const int col0 = p1 * 256 + wc * 64;
                u32x2 bb[4];
#pragma unroll
                for (int n = 0; n < 4; ++n) { int c = col0 + n * 16 + 4 * g; c = c < FF - 4 ? c : FF - 4; bb[n] = *(const u32x2*)(a.b1 + c * 2); }
                const u32 base = OFF_H + (u32)(p0 * 128 + wr * 64 + fr) * (FF * 2) + (col0 + c8) * 2;
#pragma unroll
                for (int m = 0; m < 4; ++m) {
                    u32 h[4][2];
#pragma unroll
                    for (int n = 0; n < 4; ++n) {
                        float t0 = fmaxf(bfr(bfr(acc[m][n][0]) + blo(bb[n].x)), 0.f), t1 = fmaxf(bfr(bfr(acc[m][n][1]) + bhi(bb[n].x)), 0.f);
                        float t2 = fmaxf(bfr(bfr(acc[m][n][2]) + blo(bb[n].y)), 0.f), t3 = fmaxf(bfr(bfr(acc[m][n][3]) + bhi(bb[n].y)), 0.f);
                        h[n][0] = pack2(t0 * t0, t1 * t1); h[n][1] = pack2(t2 * t2, t3 * t3);
                    }
#pragma unroll
                    for (int n = 0; n < 4; n += 2) {
                        u32x4 v = pair8(h[n][0], h[n][1], h[n + 1][0], h[n + 1][1]);
                        if (col0 < FF) bst128<16>(rws, v, base + m * 16 * (FF * 2) + n * 32);
                    }
                }
            }
            WAIT_V(0);
            RAW_BARRIER();
            if (tid == 0) flag_add(flags, F_H + p0);
        } else if (type == 3) {
            f32x4 acc[4][2];
            wait_flag(flags, F_H + p0, 7, dead);
            {
                const u32x4 r0 = mkrsrc(a.w2, W1_BYTES);
                gemm_core<2, 1>(acc, rs, OFF_H + (u32)p0 * 128u * (FF * 2), FF * 2, r0, p1 * 256, 1280, 15, 27, lds, wid, lane);
            }
            {
                const int col0 = p1 * 128 + wc * 32;
                u32x2 bb[2];
#pragma unroll
                for (int n = 0; n < 2; ++n) bb[n] = *(const u32x2*)(a.b2 + (col0 + n * 16 + 4 * g) * 2);
                const u32 base = OFF_G + (u32)(p0 * 128 + wr * 64 + fr) * 1280u + (col0 + c8) * 2;
                u32x4 sv[4], xv[4];
                if (p0 < ge) {
                    wait_flag(flags, F_S + p0, 40, dead);
#pragma unroll
                    for (int m = 0; m < 4; ++m) { sv[m] = bld128s(rws, base + m * 16 * 1280); xv[m] = *(const u32x4*)(a.x + (base - OFF_G) + m * 16 * 1280); }
                }
#pragma unroll
                for (int m = 0; m < 4; ++m) {
                    u32 h[2][2];
#pragma unroll
                    for (int n = 0; n < 2; ++n) {
                        float t[4] = {bfr(bfr(acc[m][n][0]) + blo(bb[n].x)), bfr(bfr(acc[m][n][1]) + bhi(bb[n].x)),
                                      bfr(bfr(acc[m][n][2]) + blo(bb[n].y)), bfr(bfr(acc[m][n][3]) + bhi(bb[n].y))};
#pragma unroll
                        for (int r = 0; r < 4; ++r) t[r] = t[r] / (1.0f + __expf(-t[r]));
                        h[n][0] = pack2(t[0], t[1]); h[n][1] = pack2(t[2], t[3]);
                    }
                    u32x4 v = pair8(h[0][0], h[0][1], h[1][0], h[1][1]);
                    if (p0 < ge) {
#pragma unroll
                        for (int r = 0; r < 4; ++r) v[r] = pack2(blo(xv[m][r]) + bfr(blo(sv[m][r]) * blo(v[r])), bhi(xv[m][r]) + bfr(bhi(sv[m][r]) * bhi(v[r])));
                        *(u32x4*)(a.out + (base - OFF_G) + m * 16 * 1280) = v;
                    } else bst128<16>(rws, v, base + m * 16 * 1280);
                }
            }
            WAIT_V(0);
            RAW_BARRIER();
            if (tid == 0) flag_add(flags, F_G + p0);
        } else {
            const int seg = p0, cg0 = p1 & 15, cg1 = cg0 + (p1 >> 4);
            wait_prog(rws, F_VB, wid * 4, 4, seg, known, lane, dead);
            u32x4 yb[4][4], yu[4], px[4][2], pm[2];
            auto ldb = [&](int cg) {
#pragma unroll
                for (int hh = 0; hh < 4; ++hh)
#pragma unroll
                    for (int n = 0; n < 4; ++n) yb[hh][n] = bld128s(rws, OFF_B + ((seg * 32 + wid * 4 + hh) * 40 + cg * 4 + n) * 1024u + lane * 16);
            };
            auto ldu = [&](int cg) {
#pragma unroll
                for (int n = 0; n < 4; ++n) yu[n] = bld128s(rws, OFF_U + (seg * 32 + wid * 4 + g) * 10240u + (cg * 64 + n * 16 + fr) * 16);
            };
            ldb(cg0);
            const bool first = (seg >> 2) < ge;
            if (!first) wait_flag(flags, F_G + (seg >> 2), 5, dead);
            u32 eo = (u32)(seg * 32 + (wid >> 2) * 16 + fr) * 1280u + (cg0 * 64 + (wid & 3) * 16 + 4 * g) * 2;
            u32x2 xv = {0, 0}, gv = xv;
            if (!first) { xv = *(const u32x2*)(a.x + eo); gv = __builtin_amdgcn_raw_buffer_load_b64(rws, OFF_G + eo, 0, 16); }
            if (wid == 0) wait_flag(flags, F_X + seg, FX_FULL, dead);
            RAW_BARRIER();
#pragma unroll
            for (int hh = 0; hh < 4; ++hh)
#pragma unroll
                for (int qt = 0; qt < 2; ++qt) px[hh][qt] = bld128s(rws, OFF_PX + (seg * 32 + wid * 4 + hh) * 2048u + qt * 1024 + lane * 16);
#pragma unroll
            for (int qt = 0; qt < 2; ++qt) pm[qt] = bld128s(rws, OFF_PXM + (seg * 8 + wid) * 2048u + qt * 1024 + lane * 16);
            ldu(cg0);
#pragma unroll 1
            for (int cg = cg0; cg < cg1; ++cg) {
                lptr red = lds + (cg & 1) * 65536 + lane * 16;
#pragma unroll
                for (int qt = 0; qt < 2; ++qt)
#pragma unroll
                    for (int n = 0; n < 4; ++n) {
                        f32x4 t = {0.f, 0.f, 0.f, 0.f};
#pragma unroll
                        for (int hh = 0; hh < 4; ++hh) t = mfma(yb[hh][n], px[hh][qt], t);
                        *(LDSP f32x4*)(red + (wid * 8 + qt * 4 + n) * 1024) = mfma(yu[n], pm[qt], t);
                    }
                const u32x2 x0 = xv, g0 = gv;
                SCHED();
                if (cg + 1 < cg1) {
                    ldb(cg + 1); ldu(cg + 1);
                    if (!first) { xv = *(const u32x2*)(a.x + eo + 128); gv = __builtin_amdgcn_raw_buffer_load_b64(rws, OFF_G + eo + 128, 0, 16); }
                }
                SCHED();
                RAW_BARRIER();
                f32x4 s = {0.f, 0.f, 0.f, 0.f};
#pragma unroll
                for (int w = 0; w < 8; ++w) s += *(LDSP f32x4*)(red + (w * 8 + wid) * 1024);
                u32x2 o;
                o.x = pack2(blo(x0.x) + bfr(bfr(s[0]) * blo(g0.x)), bhi(x0.x) + bfr(bfr(s[1]) * bhi(g0.x)));
                o.y = pack2(blo(x0.y) + bfr(bfr(s[2]) * blo(g0.y)), bhi(x0.y) + bfr(bfr(s[3]) * bhi(g0.y)));
                if (first) bst64<16>(rws, (u32x2){pack2(s[0], s[1]), pack2(s[2], s[3])}, OFF_G + eo);
                else *(u32x2*)(a.out + eo) = o;
                eo += 128;
            }
            if (first) {
                WAIT_V(0);
                RAW_BARRIER();
                if (tid == 0) flag_add(flags, F_S + (seg >> 2), cg1 - cg0);
            }
        }
    }
}

#define BL4 "buffer_load_dwordx4 "
template <int N> struct IC { static constexpr int value = N; };
DEV u32 umax3(u32 a, u32 b, u32 c) { return umax(umax(a, b), c); }
DEV u32x4 bld128so(rsrc_t r, u32 vo, u32 so) { return __builtin_amdgcn_raw_buffer_load_b128(r, vo, so, 16); }
DEV float dpp_ror8(float v) { return bitsf((u32)__builtin_amdgcn_update_dpp(0, (int)fbits(v), 0x128, 0xf, 0xf, true)); }
DEV int memi_unit(int k) { return k ^ (((k >> 4) & 1) << 3); }

constexpr int L_MEMI = 0, L_QKI = 10240, L_RING = 16384;

DEV int gather_mem(const Args& a, u32x4 rs, lptr lds, int s, u32 so, u32 tshot, int wid, int lane, int tid, bool& dead) {
    int n = 0;
    if (s == 0) {
        if (wid >= 3)
#pragma unroll
        for (int k = tid - 192; k < tid - 192 + DM; k += 320) {
            const unsigned short* m = (const unsigned short*)a.mem0 + k;
            lptr d = lds + L_MEMI + (k >> 4) * 256 + (((k & 15) * 8) ^ (((k >> 4) & 1) * 64));
            *(LDSP u32x2*)d = (u32x2){m[0] | ((u32)m[DM] << 16), m[2 * DM] | ((u32)m[3 * DM] << 16)};
            *(LDSP u32x2*)(d + 128) = (u32x2){m[4 * DM] | ((u32)m[5 * DM] << 16), m[6 * DM] | ((u32)m[7 * DM] << 16)};
        }
    } else if (wid >= 3) {
        u32x4 t0, t1;
        lane = opq(lane);
        const u32 o = opq((wid - 3) * 2048 + lane * 16);
        const u32 v0 = opq(blk(o));
        wait_slot(tshot);
        asm volatile("s_nop 4\n\t" BL4 "%0, %2, %3, %4 offen sc1\n\t" BL4 "%1, %2, %3, %4 offen offset:1024 sc1\n\ts_waitcnt vmcnt(0)"
                     : "=&v"(t0), "=&v"(t1) : "v"(v0), "s"(rs), "s"(so) : "memory");
        for (;;) {
            *(LDSP u32x4*)(lds + L_MEMI + o) = t0;
            *(LDSP u32x4*)(lds + L_MEMI + o + 1024) = t1;
            SCHED();
            const unsigned long long miss = __ballot(umax(max4(t0), max4(t1)) == SENT);
            if (__builtin_expect(miss == 0, 1) || dead) break;
            asm volatile("s_waitcnt lgkmcnt(0)\n\ts_mov_b64 exec, %5\n\ts_nop 4\n\t" BL4 "%0, %2, %3, %4 offen sc1\n\t" BL4 "%1, %2, %3, %4 offen offset:1024 sc1\n\t"
                         "s_mov_b64 exec, -1\n\ts_waitcnt vmcnt(0)"
                         : "+v"(t0), "+v"(t1) : "v"(v0), "s"(rs), "s"(so), "s"(miss) : "memory");
            if (++n > SPIN_CAP) dead = true;
        }
    }
    return n;
}
DEV u32 get_parity(u32* flags, bool& dead) {
    wait_flag(flags, F_PAR, 8, dead);
    return __builtin_amdgcn_readfirstlane(__hip_atomic_load(flags + F_PAR * 32, __ATOMIC_RELAXED, __HIP_MEMORY_SCOPE_AGENT));
}
DEV u32x2 get_sched(rsrc_t rws, u32 par, int copy, int s, bool& dead) {
    const u32 o = OFF_X + (par & 1u) * 8192u + blk(XP_W) + copy * 16384 + s * 128;
    for (int n = 0;; ++n) {
        asm volatile("" ::: "memory");
        const u32x2 v = __builtin_amdgcn_raw_buffer_load_b64(rws, 0, o, 16);
        const u32 x = __builtin_amdgcn_readfirstlane(v.x), y = __builtin_amdgcn_readfirstlane(v.y);
        if (n > SPIN_CAP) dead = true;
        if (dead) { const u32 t = now4(); return (u32x2){t, t}; }
        if (x != SENT && y != SENT) return (u32x2){x, y};
        __builtin_amdgcn_s_sleep(2);
    }
}
DEV int a_off(int lane) {
    const int G = lane >> 4, q = (lane >> 2) & 3, p = lane & 3;
    return 16 * memi_unit(16 * G + 4 * (q >> 1) + (q & 1) + 2 * (p >> 1)) + 8 * (p & 1);
}
DEV int a_off2(int lane) {
    const int G = lane >> 4, q = (lane >> 2) & 3, p = lane & 3;
    return G * 256 + (p & 1) * 128 + (((4 * (q >> 1) + (q & 1) + 2 * (p >> 1)) * 8) ^ ((G & 1) * 64));
}
DEV void stage_rows(u32x4 r, u32 off, int ld, int rowb, int rows, lptr dst, int wid, int lane) {
    const int total = rows * rowb / 1024;
    for (int i = wid; i < total; i += 8) {
        const int b = i * 1024 + lane * 16;
        bdma<0>(r, (u32)(b / rowb) * ld + (b % rowb), off, (u32)(size_t)dst + i * 1024);
    }
}
DEV u32x8 wfrag(lptr img, int rowb, int k0, int col0, int lane) {
    const int g = lane >> 4, q = (lane >> 2) & 3, p = lane & 3;
    lptr b = img + (k0 + 8 * g + q) * rowb + (col0 + 4 * p) * 2;
    return cat(cat(trread(b), trread(b + 4 * rowb)), cat(trread(b + 32 * rowb), trread(b + 36 * rowb)));
}

template <int NE, class F> DEV void load_wqk(const Args& a, lptr lds, int h, u32x8 (&W)[2][10], int wid, int lane, F early) {
    const u32x4 rq = mkrsrc(a.Wq, WQ_BYTES), rk = mkrsrc(a.Wk, WQ_BYTES);
    auto st = [&](int pc) {
        lptr d = lds + (pc & 1) * 65536;
        stage_rows(rq, (u32)pc * 128u * 8192u + h * 256, 8192, 256, 128, d, wid, lane);
        stage_rows(rk, (u32)pc * 128u * 8192u + h * 256, 8192, 256, 128, d + 32768, wid, lane);
    };
    st(0);
#pragma unroll
    for (int pc = 0; pc < 5; ++pc) {
        if (pc + 1 < 5) { st(pc + 1); WAIT_V(8); } else { early(); WAIT_V(NE); }
        RAW_BARRIER();
        lptr img = lds + (pc & 1) * 65536 + (wid >> 2) * 32768;
#pragma unroll
        for (int c = 0; c < 2; ++c)
#pragma unroll
            for (int t = 0; t < 2; ++t) W[t][pc * 2 + c] = wfrag(img, 256, c * 64, (wid & 3) * 32 + t * 16, lane);
        RAW_BARRIER();
    }
}
DEV void load_wvo(const Args& a, lptr lds, int h, u32x8 (&Wv)[10], u32x8 (&Wo)[5][2], int wid, int lane) {
    const u32x4 rv = mkrsrc(a.Wv, WQ_BYTES), ro = mkrsrc(a.Wo, WQ_BYTES);
    auto stv = [&](int pc) { stage_rows(rv, (u32)pc * 320u * 8192u + h * 256, 8192, 256, 320, lds + (pc & 1) * 81920, wid, lane); };
    auto sto = [&](int pc) { stage_rows(ro, (u32)(h * 128 + pc * 64) * 1280u, 1280, 1280, 64, lds + (pc & 1) * 81920, wid, lane); };
    stv(0); stv(1);
#pragma unroll
    for (int pc = 0; pc < 2; ++pc) {
        WAIT_V(10);
        RAW_BARRIER();
#pragma unroll
        for (int c = 0; c < 5; ++c) Wv[pc * 5 + c] = wfrag(lds + pc * 81920, 256, c * 64, wid * 16, lane);
        RAW_BARRIER();
        sto(pc);
    }
#pragma unroll
    for (int pc = 0; pc < 2; ++pc) {
        if (pc == 0) WAIT_V(10); else WAIT_V(0);
        RAW_BARRIER();
#pragma unroll
        for (int t = 0; t < 5; ++t) Wo[t][pc] = wfrag(lds + pc * 81920, 1280, 0, wid * 80 + t * 16, lane);
    }
    RAW_BARRIER();
}
DEV u32x4 half(const u32x8& w, int i) { return i ? (u32x4){w[4], w[5], w[6], w[7]} : (u32x4){w[0], w[1], w[2], w[3]}; }

DEV int seg_of(int i, int hf) { return 2 * i + hf; }
DEV void x_piece(u32x4 rs, u32 xv, lptr lds, int seg, int slot, int j, int wid) {
    bdma<0>(rs, xv, OFF_XN + (u32)(seg * 32 + 16 * (wid >> 2)) * 1280u + ((wid & 3) * 5 + j) * 64, (u32)(size_t)lds + slot * 40960 + (wid * 5 + j) * 1024);
}
DEV int swz64(int o) { return o ^ (((o >> 9) & 1) << 5); }

DEV void stream_qk(const Args& a, lptr lds, int h, int hf, int wid, int lane, int tid) {
    const rsrc_t rws = __builtin_amdgcn_make_buffer_rsrc(a.ws, 0, WS_USED, 0x00020000);
    const u32x4 rs = mkrsrc(a.ws, WS_USED);
    const int g = lane >> 4, fr = lane & 15;
    u32x8 W[2][10];
    const u32 xv = (u32)(swz64(lane * 16) >> 6) * 1280u + (swz64(lane * 16) & 63);
    load_wqk<5>(a, lds, h, W, wid, lane, [&]() {
#pragma unroll
        for (int j = 0; j < 5; ++j) x_piece(rs, xv, lds, seg_of(0, hf), 2, j, wid);
    });
    const int xo = swz64(fr * 64 + g * 16);
    const u32 vo = (u32)fr * 8192u + ((wid & 3) * 32 + 8 * ((g >> 1) + 2 * (g & 1))) * 2, ob = ((wid >> 2) ? OFF_K : OFF_Q) + h * 256;
#pragma unroll
    for (int j = 0; j < 5; ++j) x_piece(rs, xv, lds, seg_of(1, hf), 0, j, wid);
    int slot = 2;
#pragma unroll 1
    for (int i = 0; i < 32; ++i) {
        if (i == 0) WAIT_V(5); else if (i <= 2) WAIT_V(0); else if (i == 31) WAIT_V(2); else WAIT_V(7);
        RAW_BARRIER();
        if (wid == 0 && i < 8) TS(0, 3 + i);
        if (wid == 0 && (i == 0 || i == 7)) TSV(0, 11 + (i & 1), __builtin_amdgcn_s_memtime());
        if (tid == 0 && i) bst32<16>(rws, i <= 2 ? i : i - 1, OFF_FLAGS + (F_KQ + hf) * 128 + h * 4);
        lptr b = lds + slot * 40960 + xo;
        const int seg = seg_of(i, hf), nseg = seg_of(i + 2, hf), nslot = slot >= 1 ? slot - 1 : 2;
        u32x4 xf[8];
        f32x4 c[2][2];
#pragma unroll
        for (int j = 0; j < 7; ++j) { xf[j] = lds128(b + j * 1024); SCHED(); }
        auto put = [&](int m) {
            __builtin_amdgcn_raw_buffer_store_b128(pair8(nosent(pack2(c[m][0][0], c[m][0][1])), nosent(pack2(c[m][0][2], c[m][0][3])),
                                                         nosent(pack2(c[m][1][0], c[m][1][1])), nosent(pack2(c[m][1][2], c[m][1][3]))), rws, vo, ob + (u32)(seg * 32 + m * 16) * 8192u, 16);
        };
#pragma unroll
        for (int j = 0; j < 40; ++j) {
            const int m = j / 20, k = j % 20;
            const f32x4 z = {0.f, 0.f, 0.f, 0.f};
            c[m][0] = mfma(half(W[0][k >> 1], k & 1), xf[j & 7], k ? c[m][0] : z);
            c[m][1] = mfma(half(W[1][k >> 1], k & 1), xf[j & 7], k ? c[m][1] : z);
            SCHED();
            if (j + 7 < 40) xf[(j + 7) & 7] = lds128(b + (j + 7) * 1024);
            if (j < 20 && (j & 3) == 0 && i + 2 < 32) x_piece(rs, xv, lds, nseg, nslot, j >> 2, wid);
            if (j == 22) put(0);
            SCHED();
        }
        put(1);
        slot = slot == 2 ? 0 : slot + 1;
    }
    WAIT_V(0);
    RAW_BARRIER();
    if (tid == 0) bst32<16>(rws, 40, OFF_FLAGS + (F_KQ + hf) * 128 + h * 4);
}

DEV void stream_vb(const Args& a, lptr lds, int h, int hf, int wid, int lane, int tid) {
    const rsrc_t rws = __builtin_amdgcn_make_buffer_rsrc(a.ws, 0, WS_USED, 0x00020000);
    const u32x4 rs = mkrsrc(a.ws, WS_USED);
    const int g = lane >> 4, fr = lane & 15;
    u32x8 Wv[10], Wo[5][2];
    const u32 xv = (u32)(swz64(lane * 16) >> 6) * 1280u + (swz64(lane * 16) & 63);
    const int xo = swz64(fr * 64 + g * 16);
    const int vw = (wid >> 1) * 1024 + swz64(fr * 64 + (wid & 1) * 32 + g * 8);
    auto xseg = [&](int i, int sl) {
#pragma unroll
        for (int j = 0; j < 5; ++j) x_piece(rs, xv, lds, seg_of(i, hf), sl, j, wid);
    };
    auto vrows = [&](int sl, u32x2 (&d)[2]) {
        lptr b = lds + sl * 40960 + xo;
        u32x4 xf[8];
        f32x4 c[2];
#pragma unroll
        for (int j = 0; j < 7; ++j) { xf[j] = lds128(b + ((j & 1) * 20 + (j >> 1)) * 1024); SCHED(); }
#pragma unroll
        for (int j = 0; j < 40; ++j) {
            const int m = j & 1, k = j >> 1;
            const f32x4 z = {0.f, 0.f, 0.f, 0.f};
            c[m] = mfma(half(Wv[k >> 1], k & 1), xf[j & 7], k ? c[m] : z);
            SCHED();
            if (j + 7 < 40) xf[(j + 7) & 7] = lds128(b + (((j + 7) & 1) * 20 + ((j + 7) >> 1)) * 1024);
            SCHED();
        }
#pragma unroll
        for (int m = 0; m < 2; ++m) d[m] = (u32x2){pack2(c[m][0], c[m][1]), pack2(c[m][2], c[m][3])};
    };
    {
        const u32x4 rv = mkrsrc(a.Wv, WQ_BYTES), ro = mkrsrc(a.Wo, WQ_BYTES);
        auto sto = [&](int q, int sl) { stage_rows(ro, (u32)(h * 128 + q * 32) * 1280u, 1280, 1280, 32, lds + sl * 40960, wid, lane); };
        auto rdo = [&](int q, int sl) {
            lptr b = lds + sl * 40960 + (8 * g + ((lane >> 2) & 3)) * 1280 + (wid * 80 + 4 * (lane & 3)) * 2;
#pragma unroll
            for (int t = 0; t < 5; ++t) {
                const u32x2 lo = trread(b + t * 32), hi = trread(b + t * 32 + 4 * 1280);
                Wo[t][q >> 1][(q & 1) * 4 + 0] = lo.x; Wo[t][q >> 1][(q & 1) * 4 + 1] = lo.y;
                Wo[t][q >> 1][(q & 1) * 4 + 2] = hi.x; Wo[t][q >> 1][(q & 1) * 4 + 3] = hi.y;
            }
        };
#pragma unroll
        for (int pc = 0; pc < 2; ++pc) stage_rows(rv, (u32)pc * 320u * 8192u + h * 256, 8192, 256, 320, lds + pc * 81920, wid, lane);
#pragma unroll
        for (int pc = 0; pc < 2; ++pc) {
            WAIT_V(10);
            RAW_BARRIER();
#pragma unroll
            for (int c = 0; c < 5; ++c) Wv[pc * 5 + c] = wfrag(lds + pc * 81920, 256, c * 64, wid * 16, lane);
            RAW_BARRIER();
            if (pc == 0) { xseg(0, 0); sto(0, 1); } else { sto(1, 2); sto(2, 3); }
        }
        u32x2 d[2];
        WAIT_V(15); RAW_BARRIER();
        vrows(0, d);
        WAIT_V(10); RAW_BARRIER();
        rdo(0, 1);
        RAW_BARRIER();
        sto(3, 0); xseg(1, 1);
        WAIT_V(15); RAW_BARRIER();
        rdo(1, 2);
        WAIT_V(10); RAW_BARRIER();
        rdo(2, 3);
        RAW_BARRIER();
        xseg(2, 2);
#pragma unroll
        for (int m = 0; m < 2; ++m) *(LDSP u32x2*)(lds + 122880 + m * 4096 + vw) = d[m];
        WAIT_V(10); RAW_BARRIER();
        rdo(3, 0);
    }
    int slot = 1;
#pragma unroll 1
    for (int i = 1; i <= 32; ++i) {
        if (i == 1) {} else if (i >= 31) WAIT_V(5); else if (i <= 3) WAIT_V(0); else WAIT_V(10);
        RAW_BARRIER();
        if (wid == 0 && i < 8) TS(0, 3 + i);
        if (tid == 0 && i >= 3) bst32<16>(rws, i <= 3 ? i - 1 : i - 2, OFF_FLAGS + (F_VB + hf) * 128 + h * 4);
        if (i > 1 && i + 2 < 32) xseg(i + 2, slot >= 1 ? slot - 1 : 2);
#pragma unroll 1
        for (int ph = 0; ph < 2; ++ph) {
            if (ph != (i <= 2 ? 0 : wid >> 2)) {
                if (i == 32) continue;
                if (i == 1) {
                    WAIT_V(0);
                    RAW_BARRIER();
                    if (wid == 0) TS(0, 3);
                    if (tid == 0) bst32<16>(rws, 1, OFF_FLAGS + (F_VB + hf) * 128 + h * 4);
                    xseg(3, 0);
                }
                u32x2 d[2];
                vrows(slot, d);
#pragma unroll
                for (int m = 0; m < 2; ++m) *(LDSP u32x2*)(lds + 122880 + (i & 1) * 8192 + m * 4096 + vw) = d[m];
            } else {
                lptr b = lds + 122880 + ((i - 1) & 1) * 8192 + xo;
                const u32 so = OFF_B + (u32)((seg_of(i - 1, hf) * 32 + h) * 40 + wid * 5) * 1024u;
                u32x4 vf[2][4];
#pragma unroll
                for (int j = 0; j < 8; ++j) { vf[j & 1][j >> 1] = lds128(b + (j & 1) * 4096 + (j >> 1) * 1024); SCHED(); }
                f32x4 c[2][2];
#pragma unroll
                for (int t = 0; t <= 5; ++t) {
#pragma unroll
                    for (int k = 0; k < 4; ++k) {
                        const f32x4 z = {0.f, 0.f, 0.f, 0.f};
                        if (t < 5) {
                            c[t & 1][0] = mfma(vf[0][k], half(Wo[t][k >> 1], k & 1), k ? c[t & 1][0] : z);
                            c[t & 1][1] = mfma(vf[1][k], half(Wo[t][k >> 1], k & 1), k ? c[t & 1][1] : z);
                        }
                        SCHED();
                        if (t && k == 1) {
                            const f32x4 (&d)[2] = c[(t - 1) & 1];
                            __builtin_amdgcn_raw_buffer_store_b128((u32x4){pack2(d[0][0], d[0][1]), pack2(d[0][2], d[0][3]), pack2(d[1][0], d[1][1]), pack2(d[1][2], d[1][3])},
                                                                   rws, lane * 16, so + (t - 1) * 1024, 16);
                        }
                        SCHED();
                    }
                }
            }
        }
        slot = slot == 2 ? 0 : slot + 1;
    }
    WAIT_V(0);
    RAW_BARRIER();
    if (tid == 0) bst32<16>(rws, 40, OFF_FLAGS + (F_VB + hf) * 128 + h * 4);
}

DEV void chain_s(const Args& a, lptr lds, int h, int wid, int lane, int tid) {
    char* ws = a.ws;
    u32* flags = (u32*)(ws + OFF_FLAGS);
    const rsrc_t rws = __builtin_amdgcn_make_buffer_rsrc(ws, 0, WS_USED, 0x00020000);
    const u32x4 rs = mkrsrc(ws, WS_USED);
    const int g = lane >> 4, fr = lane & 15;
    bool dead = false;
    u32x8 W[2][10];
    load_wqk<0>(a, lds, h, W, wid, lane, []() {});
    gather_mem(a, rs, lds, 0, 0, 0, wid, lane, tid, dead);
    const u32 ldsb = (u32)(size_t)lds;
    auto dma = [&](int seg, int slot, int kq) {
        const u32 d = ldsb + L_RING + slot * 16384 + (kq - 1) * 8192;
        const int ln = opq(lane), f = ln & 15;
        const u32 vo = (kq == 1 ? 16 * ((f & 3) >> 1) + 8 * (f >> 3) + 2 * ((f >> 2) & 1) + (f & 1) : f) * 8192 + (ln >> 4) * 16;
        const u32 sb = (kq == 1 ? OFF_K : OFF_Q) + (u32)seg * 32u * 8192u + h * 256, ts = (kq == 1 ? 4 : 16) * 8192;
#pragma unroll
        for (int t = 0; t < 2; ++t)
#pragma unroll
            for (int c = 0; c < 4; ++c) bdma<1>(rs, vo, sb + t * ts + c * 64, d + (t * 4 + c) * 1024);
    };
    u32 par = 0;
    int known = 0;
    if (wid != 1 && wid != 2) par = get_parity(flags, dead);
    else {
        for (int n = 0; n < SPIN_CAP; ++n) {
            dma(0, 0, wid); dma(1, 1, wid);
            WAIT_V(0);
            u32 mx = 0;
#pragma unroll
            for (int i = 0; i < 16; ++i)
                mx = umax(mx, max4(*(volatile LDSP u32x4*)(lds + L_RING + (i >> 3) * 16384 + (wid - 1) * 8192 + (i & 7) * 1024 + lane * 16)));
            if (__ballot(mx == SENT) == 0) break;
        }
    }
    RAW_BARRIER();
    const int idx = (fr < 8) ? 0x4444 : 0xEEEE;
    const int ao = a_off2(lane);
    const float cs = 0.08838834764831845f * 1.4426950408889634f;
    TC_DECL;
    auto project = [&](auto nw) {
        constexpr int NW = decltype(nw)::value;
        RAW_BARRIER();
        TC(2);
        u32x4 av[10];
#pragma unroll
        for (int c = 0; c < NW; ++c) { av[c] = cat(trread(lds + L_MEMI + c * 1024 + ao), trread(lds + L_MEMI + c * 1024 + (ao ^ 64))); SCHED(); }
        f32x4 c0 = {0.f, 0.f, 0.f, 0.f}, c1 = c0;
        SCHED();
#pragma unroll
        for (int c = 0; c < 10; ++c) {
            c0 = smfmac(av[c], W[0][c], c0, idx);
            c1 = smfmac(av[c], W[1][c], c1, idx);
            SCHED();
            if (c + NW < 10) av[c + NW] = cat(trread(lds + L_MEMI + (c + NW) * 1024 + ao), trread(lds + L_MEMI + (c + NW) * 1024 + (ao ^ 64)));
            SCHED();
        }
        WAIT_V(0);
        const u32 d0 = pack2(fold32(c0[0], c1[0]), fold32(c0[1], c1[1])), d1 = pack2(fold32(c0[2], c1[2]), fold32(c0[3], c1[3]));
        LDSP u32* qp = (LDSP u32*)(lds + L_QKI + (wid * 32 + (lane >> 5) * 16 + fr) * 16 + (g & 1) * 4);
        qp[0] = d0; qp[2] = d1;
        RAW_BARRIER();
        TC(3);
    };
    if (wid >= 3) {
        u32 tshot = 0;
        for (int s = 0; s < NSEG; ++s) {
            TC(0);
            int nre = 0;
            if (s) nre = gather_mem(a, rs, lds, s, xstep(s, par), tshot, wid, lane, tid, dead);
            TCV(9, nre); TC(1);
            project(IC<7>());
            if (wid == 3 && s + 1 == NSEG && lane == 0) flag_add(flags, F_X + s - 1, 2);
            if (s + 1 < NSEG) tshot = get_sched(rws, par, wid - 3, s, dead).y;
            TC_FLUSH(s);
        }
        TC_DUMP();
        return;
    }
    lptr qk = lds + L_QKI + (8 * g + ((lane >> 2) & 3)) * 16 + 8 * (lane & 1);
    int slot = 0;
    if (wid == 0) {
        const int qi = 4 * ((fr >> 1) & 1) + 2 * ((fr >> 2) & 1) + (fr & 1);
        const u32 vpx = blk(XP_PX + h * 512u) + (8 * g + qi) * 16 + (fr >> 3) * 8;
        const u32 vpm = blk(XP_PM + (h >> 1) * 256u) + qi * 32 + (g & 1) * 16 + (h & 1) * 8 + (g >> 1) * 4;
        u32x4 kx[8];
        u32 pv = 0;
#pragma unroll
        for (int i = 0; i < 8; ++i) kx[i] = lds128(lds + L_RING + i * 1024 + opq(lane) * 16);
        for (int s = 0; s < NSEG; ++s) {
            const u32 so = xstep(s, par);
            project(IC<7>());
            if (s + 1 == NSEG) break;
            u32x4 qf[4], km[4];
#pragma unroll
            for (int c = 0; c < 4; ++c) {
                qf[c] = cat(trread(qk + c * 512), trread(qk + c * 512 + 64));
                km[c] = cat(trread(qk + 2048 + c * 512), trread(qk + 2048 + c * 512 + 64));
                SCHED();
            }
            f32x4 s0 = {0.f, 0.f, 0.f, 0.f}, s1 = s0, s2 = s0;
#pragma unroll
            for (int c = 0; c < 4; ++c) {
                s0 = mfma(kx[c], qf[c], s0);
                s1 = mfma(kx[4 + c], qf[c], s1);
                s2 = mfma(km[c], qf[c], s2);
                asm volatile("" : "+v"(s2));
                SCHED();
            }
            f32x2 x[3];
            x[0] = fr >= 8 ? (f32x2){s1[0], s1[1]} : (f32x2){s0[0], s0[1]};
            x[1] = fr >= 8 ? (f32x2){s1[2], s1[3]} : (f32x2){s0[2], s0[3]};
            x[2] = g >= 2 ? (f32x2){s2[2], s2[3]} : (f32x2){s2[0], s2[1]};
            float m, t;
            const f32x2 cs2 = {cs, cs};
            asm("v_max3_f32 %0, %5, %6, %7\n\tv_max3_f32 %1, %8, %9, %10\n\tv_max_f32 %0, %0, %1\n\tv_pk_mul_f32 %2, %2, %11\n\ts_nop 0\n\t"
                "v_max_f32_dpp %0, %0, %0 row_ror:8 row_mask:0xf bank_mask:0xf\n\tv_mov_b32 %1, %0\n\tv_pk_mul_f32 %3, %3, %11\n\ts_nop 0\n\t"
                "v_permlane32_swap_b32 %0, %1\n\tv_max_f32 %0, %0, %1\n\tv_mov_b32 %1, %0\n\tv_pk_mul_f32 %4, %4, %11\n\ts_nop 0\n\t"
                "v_permlane16_swap_b32 %0, %1\n\tv_max_f32 %0, %0, %1"
                : "=&v"(m), "=&v"(t), "+v"(x[0]), "+v"(x[1]), "+v"(x[2])
                : "v"(x[0].x), "v"(x[0].y), "v"(x[1].x), "v"(x[1].y), "v"(x[2].x), "v"(x[2].y), "s"(cs2));
            const float mc = m * cs;
#pragma unroll
            for (int r = 0; r < 3; ++r) {
                x[r] -= (f32x2){mc, mc};
                x[r].x = __builtin_amdgcn_exp2f(x[r].x); x[r].y = __builtin_amdgcn_exp2f(x[r].y);
            }
            const f32x2 ps = __builtin_elementwise_fma(x[2], (f32x2){0.5f, 0.5f}, x[0] + x[1]);
            float sum = ps.x + ps.y;
            sum += dpp_ror8(sum);
            sum = xsum(sum);
            const float inv = __builtin_amdgcn_rcpf(sum);
#pragma unroll
            for (int r = 0; r < 3; ++r) x[r] *= (f32x2){inv, inv};
            __builtin_amdgcn_raw_buffer_store_b64((u32x2){nosent(pack2(x[0].x, x[0].y)), nosent(pack2(x[1].x, x[1].y))}, rws, vpx, so, 16);
            __builtin_amdgcn_raw_buffer_store_b32(nosent(pack2(x[2].x, x[2].y)), rws, vpm, so, 16);
            TC(4);
            SCHED();
            __builtin_amdgcn_raw_buffer_store_b64((u32x2){SENT, SENT}, rws, vpx, xstep(s + 2, par), 16);
            __builtin_amdgcn_raw_buffer_store_b32(SENT, rws, vpm, xstep(s + 2, par), 16);
            SCHED();
            if (s + 2 < NSEG) {
                const int k0 = __builtin_amdgcn_readlane(pv, 0), k1 = __builtin_amdgcn_readlane(pv, 32), k = k0 < k1 ? k0 : k1;
                known = k > known ? k : known;
                wait_prog(rws, F_KQ, h, 1, s + 2, known, lane, dead);
                dma(s + 2, slot == 0 ? 2 : slot - 1, 1);
                dma(s + 2, slot == 0 ? 2 : slot - 1, 2);
                if (known <= s + 7) pv = prog_load(rws, F_KQ, h, 1, lane);
            }
            slot = slot == 2 ? 0 : slot + 1;
#pragma unroll
            for (int i = 0; i < 8; ++i) kx[i] = lds128(lds + L_RING + slot * 16384 + i * 1024 + lane * 16);
            TC_FLUSH(s);
        }
        TC_DUMP();
        return;
    }
    u32x4 kx[8];
#pragma unroll
    for (int i = 0; i < 8; ++i) kx[i] = lds128(lds + L_RING + i * 1024 + opq(lane) * 16);
    for (int s = 0; s < NSEG; ++s) {
        project(IC<6>());
        __builtin_amdgcn_s_sleep(2);
        const int ln = opq(lane), fr = ln & 15, g = ln >> 4;
        lptr rg = lds + L_RING + slot * 16384 + 8192 + (wid - 1) * 4096 + ln * 16;
        u32x4 qf[4], km[4];
#pragma unroll
        for (int c = 0; c < 4; ++c) {
            qf[c] = lds128(rg + c * 1024);
            km[c] = cat(trread(qk + 2048 + c * 512), trread(qk + 2048 + c * 512 + 64));
        }
        SCHED();
        f32x4 st[3];
#pragma unroll
        for (int t = 0; t < 3; ++t) st[t] = (f32x4){0.f, 0.f, 0.f, 0.f};
#pragma unroll
        for (int c = 0; c < 4; ++c) {
            st[0] = mfma(kx[c], qf[c], st[0]);
            st[1] = mfma(kx[4 + c], qf[c], st[1]);
            st[2] = mfma(km[c], qf[c], st[2]);
        }
        SCHED();
        slot = slot == 2 ? 0 : slot + 1;
#pragma unroll
        for (int i = 0; i < 8; ++i) kx[i] = lds128(lds + L_RING + slot * 16384 + i * 1024 + ln * 16);
        const float NEG = -3.0e38f;
        const int tq = (wid - 1) * 16 + fr;
#pragma unroll
        for (int t = 0; t < 2; ++t)
#pragma unroll
            for (int r = 0; r < 4; ++r) {
                const int key = 16 * (r >> 1) + 8 * (g >> 1) + 4 * t + 2 * (g & 1) + (r & 1);
                st[t][r] = key <= tq ? st[t][r] : NEG;
            }
        float m = fmaxf(fmaxf(fmaxf(st[0][0], st[0][1]), fmaxf(st[0][2], st[0][3])), fmaxf(fmaxf(st[1][0], st[1][1]), fmaxf(st[1][2], st[1][3])));
        m = fmaxf(m, fmaxf(fmaxf(st[2][0], st[2][1]), fmaxf(st[2][2], st[2][3])));
        m = xmax(m);
        float sum = 0.f;
#pragma unroll
        for (int t = 0; t < 3; ++t)
#pragma unroll
            for (int r = 0; r < 4; ++r) {
                const float e = __builtin_amdgcn_exp2f((st[t][r] - m) * cs);
                st[t][r] = e;
                sum += (t == 2 && g >= 2) ? 0.f : e;
            }
        sum = xsum(sum);
        const float inv = __builtin_amdgcn_rcpf(sum);
        u32 d[3][2];
#pragma unroll
        for (int t = 0; t < 3; ++t) { d[t][0] = pack2(st[t][0] * inv, st[t][1] * inv); d[t][1] = pack2(st[t][2] * inv, st[t][3] * inv); }
        swap16(d[0][0], d[1][0]); swap16(d[0][1], d[1][1]);
        bst128<16>(rws, (u32x4){d[0][0], d[1][0], d[0][1], d[1][1]}, OFF_PX + (u32)(s * 32 + h) * 2048u + (wid - 1) * 1024 + ln * 16);
        u32 e0 = d[2][0], e1 = d[2][1];
        swap16(d[2][0], e0); swap16(d[2][1], e1);
        if (g == 0) bst128<16>(rws, (u32x4){d[2][0], e0, d[2][1], e1}, OFF_PXM + (u32)(s * 8 + (h >> 2)) * 2048u + (wid - 1) * 1024 + ((h & 3) * 16 + fr) * 16);
        if (s + 1 == NSEG) {
            WAIT_V(0);
            if (lane == 0) flag_add(flags, F_X + s);
        }
        TC(4);
        TC_FLUSH(s);
    }
    TC_DUMP();
}

DEV void chain_v(const Args& a, lptr lds, int h, int wid, int lane, int tid) {
    char* ws = a.ws;
    u32* flags = (u32*)(ws + OFF_FLAGS);
    const rsrc_t rws = __builtin_amdgcn_make_buffer_rsrc(ws, 0, WS_USED, 0x00020000);
    const u32x4 rs = mkrsrc(ws, WS_USED);
    const int g = lane >> 4, fr = lane & 15;
    bool dead = false;
    u32x8 Wv[10], Wo[5][2];
    load_wvo(a, lds, h, Wv, Wo, wid, lane);
    const u32 par = get_parity(flags, dead);
    u32 tshot = 0;
    TC_DECL;
    const int idx = (fr < 8) ? 0x4444 : 0xEEEE;
    const int ao = a_off(lane), ao2 = a_off2(lane);
    const u32 vr0 = blk(XP_U) + (wid * 5 + (g & 1) * 2 + (g >> 1)) * 16384u + h * 256 + fr * 16;
    const u32 vr1 = g == 0 ? blk(XP_U) + (wid * 5 + 4) * 16384u + h * 256 + fr * 16 : 0x80000000u;
    for (int s = 0; s < NSEG; ++s) {
        TC(0);
        if (s == 1 && wid >= 3) { int known = 0; wait_prog(rws, F_VB, 0, 32, 0, known, lane, dead, 8); }
        const int nre = gather_mem(a, rs, lds, s, xstep(s, par), tshot, wid, lane, tid, dead);
        TCV(9, nre); TC(1);
        f32x4 c0 = {0.f, 0.f, 0.f, 0.f};
        asm volatile("" : "+v"(c0));
        RAW_BARRIER();
        TC(2);
        f32x4 u[5];
        {
            u32x4 av[10];
#pragma unroll
            for (int c = 0; c < 10; ++c) { av[c] = cat(trread(lds + L_MEMI + c * 1024 + ao2), trread(lds + L_MEMI + c * 1024 + (ao2 ^ 64))); SCHED(); }
#pragma unroll
            for (int c = 0; c < 10; ++c) {
                c0 = smfmac(av[c], Wv[c], c0, idx);
                if (c >= 5) { u[c - 5] = (f32x4){0.f, 0.f, 0.f, 0.f}; asm volatile("" : "+v"(u[c - 5])); }
                SCHED();
            }
            const u32 d = pack2(fold32(c0[0], c0[2]), fold32(c0[1], c0[3]));
            *(LDSP u32*)(lds + L_QKI + memi_unit(wid * 16 + fr) * 16 + (2 * (g & 1) + (g >> 1)) * 4) = d;
        }
        WAIT_V(0);
        const u32 so = xstep(s, par);
        RAW_BARRIER();
        TC(3);
        {
            u32x4 av[2];
#pragma unroll
            for (int c = 0; c < 2; ++c) { av[c] = cat(trread(lds + L_QKI + c * 1024 + ao), trread(lds + L_QKI + c * 1024 + (ao ^ 128))); SCHED(); }
            u[4] = smfmac(av[0], Wo[4][0], u[4], idx); u[0] = smfmac(av[0], Wo[0][0], u[0], idx); SCHED();
            u[4] = smfmac(av[1], Wo[4][1], u[4], idx); u[1] = smfmac(av[0], Wo[1][0], u[1], idx); SCHED();
            u[0] = smfmac(av[1], Wo[0][1], u[0], idx); u[1] = smfmac(av[1], Wo[1][1], u[1], idx); SCHED();
            u32 e0 = nosent(pack2(fold32(u[4][0], u[4][2]), fold32(u[4][1], u[4][3]))), e1 = e0;
            u[2] = smfmac(av[0], Wo[2][0], u[2], idx); SCHED();
            swap32(e0, e1);
            u32 f0 = e0, f1 = e1;
            u[3] = smfmac(av[0], Wo[3][0], u[3], idx); SCHED();
            swap16(e0, f0); swap16(e1, f1);
            const u32x4 d1 = {e0, e1, f0, f1};
            __builtin_amdgcn_raw_buffer_store_b128(d1, rws, vr1, so, 16);
            u[2] = smfmac(av[1], Wo[2][1], u[2], idx); SCHED();
            u32 x0 = nosent(pack2(fold32(u[0][0], u[1][0]), fold32(u[0][1], u[1][1])));
            u[3] = smfmac(av[1], Wo[3][1], u[3], idx); SCHED();
            u32 x1 = nosent(pack2(fold32(u[0][2], u[1][2]), fold32(u[0][3], u[1][3])));
            SCHED();
            u32 y0 = nosent(pack2(fold32(u[2][0], u[3][0]), fold32(u[2][1], u[3][1]))), y1 = nosent(pack2(fold32(u[2][2], u[3][2]), fold32(u[2][3], u[3][3])));
            swap16(x0, y0); swap16(x1, y1);
            const u32x4 d0 = {x0, x1, y0, y1};
            __builtin_amdgcn_raw_buffer_store_b128(d0, rws, vr0, so, 16);
            TC(4);
            SCHED();
            __builtin_amdgcn_s_barrier();
            SCHED();
            const u32 ub = OFF_U + (u32)(s * 32 + h) * 10240u + wid * 1280, sa = xstep(s + 2, par);
            const u32x4 sent = {SENT, SENT, SENT, SENT};
            bst128<16>(rws, d0, ub + (((g & 1) * 2 + (g >> 1)) * 16 + fr) * 16);
            if (g == 0) bst128<16>(rws, d1, ub + (64 + fr) * 16);
            __builtin_amdgcn_raw_buffer_store_b128(sent, rws, vr0, sa, 16);
            __builtin_amdgcn_raw_buffer_store_b128(sent, rws, vr1, sa, 16);
        }
        if (wid >= 3 && s + 1 < NSEG) tshot = get_sched(rws, par, wid - 3, s, dead).y;
        if (s + 1 == NSEG) {
            if (tid == 0) flag_add(flags, F_X + s - 1);
            WAIT_V(0);
            RAW_BARRIER();
            if (tid == 0) flag_add(flags, F_X + s);
        }
        TC_FLUSH(s);
    }
    TC_DUMP();
}

DEV void chain_c(const Args& a, lptr lds, int tile, int wid, int lane, int tid) {
    char* ws = a.ws;
    u32* flags = (u32*)(ws + OFF_FLAGS);
    const rsrc_t rws = __builtin_amdgcn_make_buffer_rsrc(ws, 0, WS_USED, 0x00020000);
    const u32x4 rs = mkrsrc(ws, WS_USED);
    const int g = lane >> 4, fr = lane & 15;
    bool dead = false;
    const u32 ldsb = (u32)(size_t)lds;
    const u32 par = get_parity(flags, dead);
    TC_DECL;
    if (wid >= 4) {
        int known = 0;
        for (int s = 0; s < NSEG - 1; ++s) {
            wait_prog(rws, F_VB, (wid - 4) * 8, 8, s, known, lane, dead);
            const u32 sb = OFF_B + ((u32)(s * 32 + (wid - 4) * 8) * 40u + tile) * 1024u, d = ldsb + (s & 1) * 32768 + (wid - 4) * 8192;
#pragma unroll
            for (int i = 0; i < 8; ++i) bdma<1>(rs, lane * 16, sb + i * 40960, d + i * 1024);
            WAIT_V(0);
            u32 pv = 0;
            if (known <= s + 1) pv = prog_load(rws, F_VB, (wid - 4) * 8, 8, lane);
            RAW_BARRIER();
            if (known <= s + 1) prog_eval(pv, s + 1, known);
        }
    } else if (wid > 0) {
        for (int s = 0; s < NSEG - 1; ++s) RAW_BARRIER();
    } else {
        constexpr int NB = 24;
        const int idx = (fr < 8) ? 0x4444 : 0xEEEE;
        const u32 vx = lane * 16, vm = g * 256 + (fr & 7) * 32 + (fr >> 3) * 16;
        const u32 vst = lane < 32 ? blk(tile * 256u) + g * 128 + ((fr * 8) ^ ((tile & 1) * 64)) : 0x80000000u;
        u32 tw = now4();
        for (int s = 0; s < NSEG - 1; ++s) {
            RAW_BARRIER();
            TC(0);
            lptr rg = lds + (s & 1) * 32768 + lane * 16;
            const u32 so = xstep(s, par), su = so + blk(XP_U) + tile * 16384u;
            u32 sn = xstep(s + 1, par), sp[5] = {so + 0x5000u, so + 0x8000u, so + 0x9000u, so + 0xc000u, so + 0xd000u};
            asm volatile("" : "+s"(sn), "+s"(sp[0]), "+s"(sp[1]), "+s"(sp[2]), "+s"(sp[3]), "+s"(sp[4]));
            f32x4 c0;
            TCV(9, 0); TCV(8, 0);
#pragma unroll 1
            for (int n = 0;; ++n) {
                u32x4 yb[32], p[20];
                u32x8 yw[4];
                asm volatile("" ::: "memory");
#pragma unroll
                for (int i = 0; i < NB; ++i) yb[i] = lds128(rg + i * 1024);
                c0 = (f32x4){0.f, 0.f, 0.f, 0.f};
                asm volatile("" : "+v"(c0));
                SCHED();
                if (n == 0) {
                    wait_slot(tw);
                    const u32x2 sch = get_sched(rws, par, 5, s, dead);
                    tw = sch.y + a.fw;
                    TC(7);
                    TC(1);
                    wait_slot(sch.x);
                    TC(3);
                }
                SCHED();
#pragma unroll
                for (int i = 0; i < 20; ++i) {
                    p[i] = bld128so(rws, (i < 16 ? vx : vm) + (i & 3) * 1024, sp[i >> 2]);
                    SCHED();
                    const int j = i & 3;
                    if (i >= 16) yw[j] = cat(bld128so(rws, vx + (j & 1) * 2048, su + (j >> 1) * 4096), bld128so(rws, vx + (j & 1) * 2048 + 1024, su + (j >> 1) * 4096));
                    SCHED();
                }
                u32 mx = 0, mu = 0;
                TC(4);
                SCHED();
#pragma unroll
                for (int i = 0; i < 20; ++i) {
                    c0 = smfmac(p[i], i < 16 ? cat(yb[2 * (i & 15)], yb[2 * (i & 15) + 1]) : yw[i & 3], c0, idx);
                    mx = umax3(umax3(mx, p[i].x, p[i].y), p[i].z, p[i].w);
                    if (i >= 16) {
#pragma unroll
                        for (int j = 0; j < 4; ++j) mu = umax3(mu, yw[i & 3][j * 2], yw[i & 3][j * 2 + 1]);
                    }
                    if (i == 0) TC(5);
                    if (i == 15) TC(6);
                    if (i < (32 - NB) / 2) { yb[NB + 2 * i] = lds128(rg + (NB + 2 * i) * 1024); yb[NB + 2 * i + 1] = lds128(rg + (NB + 2 * i + 1) * 1024); }
                    SCHED();
                }
                if (__builtin_expect(__ballot(umax(mx, mu) == SENT) == 0, 1) || dead) break;
                if (n == 0) { TCV(9, __ballot(mx == SENT) != 0); TCV(8, __ballot(mu == SENT) != 0); }
                if (n > SPIN_CAP) dead = true;
            }
            u32 e0 = nosent(pack2(fold32(c0[0], c0[2]), fold32(c0[1], c0[3]))), e1 = e0;
            swap32(e0, e1);
            __builtin_amdgcn_raw_buffer_store_b64((u32x2){e0, e1}, rws, vst, sn, 16);
            TC(2);
            SCHED();
            __builtin_amdgcn_raw_buffer_store_b64((u32x2){SENT, SENT}, rws, vst, xstep(s + 3, par), 16);
            TC_FLUSH(s);
        }
    }
    TC_DUMP();
}

DEV void conductor(const Args& a, lptr lds, int wid, int lane, int tid) {
    char* ws = a.ws;
    u32* flags = (u32*)(ws + OFF_FLAGS);
    const rsrc_t rws = __builtin_amdgcn_make_buffer_rsrc(ws, 0, WS_USED, 0x00020000);
    bool dead = false;
    TC_DECL;
    if (wid == 0) {
        u32 best[4] = {~0u, ~0u, ~0u, ~0u};
        for (int k = 0; k < 6; ++k)
#pragma unroll
            for (int c = 0; c < 4; ++c) {
                const u32 c0 = cyc();
                WAIT_L0();
                if (lane == 0) bst32<16>(rws, SENT, OFF_X + (c >> 1) * 2 * MB + (c & 1) * 8192u + (56 + k) * 16384u);
                WAIT_V(0);
                const u32 d = cyc() - c0;
                best[c] = d < best[c] ? d : best[c];
            }
        const u32 par = 8u | (best[1] < best[0] ? 1u : 0u) | (best[3] < best[2] ? 2u : 0u);
        if (lane == 0) __hip_atomic_store(flags + F_PAR * 32, par, __ATOMIC_RELAXED, __HIP_MEMORY_SCOPE_AGENT);
        const u32 vw = lane < 6 ? OFF_X + (par & 1u) * 8192u + blk(XP_W) + lane * 16384 : 0x80000000u;
        u32 ts = now4();
        __builtin_amdgcn_raw_buffer_store_b64((u32x2){nosent(ts), nosent(ts)}, rws, vw, 0, 16);
        int known = 0;
        wait_prog(rws, F_VB, 0, 32, 0, known, lane, dead);
        for (int s = 1; s < NSEG; ++s) {
            const u32 so = xstep(s, par);
            TC(0);
            wait_slot(ts - a.fu);
            int n = 0;
            for (;; ++n) {
                u32x4 m[10];
                asm volatile("" ::: "memory");
#pragma unroll
                for (int i = 0; i < 10; ++i) m[i] = bld128so(rws, lane * 16 + (i & 3) * 1024, so + (i < 8 ? (i >> 2) * 4096 : 16384));
                u32 mx = 0;
#pragma unroll
                for (int i = 0; i < 10; ++i) mx = umax3(umax3(mx, m[i].x, m[i].y), m[i].z, m[i].w);
                if (__ballot(mx == SENT) == 0 || dead) break;
                if (n > SPIN_CAP) dead = true;
            }
            const u32 late = now4() - (n ? a.r0 : a.l1);
            if ((int)(late - ts) > 0) ts = late;
            const u32 tp = ts + a.fp;
            ts += a.T;
            __builtin_amdgcn_raw_buffer_store_b64((u32x2){nosent(tp), nosent(ts)}, rws, vw, s * 128, 16);
            TC(1); TCV(9, n);
            TC_FLUSH(s);
        }
    } else if (wid == 1) {
        const u32 par = get_parity(flags, dead);
        u32 tw = now4();
        for (int s = 2; s < NSEG; ++s) {
            wait_slot(tw);
            tw = get_sched(rws, par, 5, s, dead).y + a.fw;
            if (lane == 0) bst32<16>(rws, FX_FULL, OFF_FLAGS + (F_X + s - 2) * 128);
        }
    }
    TC_DUMP();
}

__global__ void __launch_bounds__(512, 2) main_kernel(Args a) {
    __shared__ __attribute__((aligned(1024))) char smem[163840];
    lptr lds = (lptr)smem;
    const int tid = threadIdx.x, lane = tid & 63, wid = __builtin_amdgcn_readfirstlane(tid >> 6);
    a.ws = ws_base(a.ws);
    const int x = blockIdx.x & 7, t = blockIdx.x >> 3;
    if (x >= 4) {
        if (wid == 0) { TS(0, 0); TS(0, 1); TSV(0, 2, ((t >> 1) & 1) * 65536 + 1); }
        if (t & 2) stream_vb(a, lds, (x - 4) * 8 + (t >> 2), t & 1, wid, lane, tid);
        else stream_qk(a, lds, (x - 4) * 8 + (t >> 2), t & 1, wid, lane, tid);
        worker(a, lds, (LDSP int*)(lds + 163824), 1, wid, lane, tid);
    }
    else if (t < 8) chain_s(a, lds, x * 8 + t, wid, lane, tid);
    else if (t < 16) chain_v(a, lds, x * 8 + t - 8, wid, lane, tid);
    else if (t < 26) chain_c(a, lds, x * 10 + t - 16, wid, lane, tid);
    else if (t == 26 && x == 0) conductor(a, lds, wid, lane, tid);
    else if (t >= 27) {
        const rsrc_t rws = __builtin_amdgcn_make_buffer_rsrc(a.ws, 0, WS_USED, 0x00020000);
        u32 v = 0;
        for (u32 j = (t - 27) * 512 + tid; j < WS_USED / 65536; j += 2560) v ^= __builtin_amdgcn_raw_buffer_load_b32(rws, j * 65536u, 0, 16);
        asm volatile("" ::"v"(v));
        for (u32 j = ((t - 27) * 4 + x) * 512 + tid; j < W1_BYTES / 16; j += 20 * 512) {
            const u32x4 w = ((const u32x4*)a.w1)[j] ^ ((const u32x4*)a.w2)[j];
            asm volatile("" ::"v"(w));
        }
    }
}

extern "C" void kernel_launch(void* const* d_in, const int* in_sizes, int n_in, void* d_out, int out_size, void* d_ws, size_t ws_size,
                              hipStream_t stream) {
    Args a;
    a.x = (const char*)d_in[0]; a.mem0 = (const char*)d_in[1]; a.Wq = (const char*)d_in[2]; a.Wk = (const char*)d_in[3];
    a.Wv = (const char*)d_in[4]; a.Wo = (const char*)d_in[5]; a.w1 = (const char*)d_in[8]; a.b1 = (const char*)d_in[9];
    a.w2 = (const char*)d_in[10]; a.b2 = (const char*)d_in[11];
    a.out = (char*)d_out; a.ws = (char*)d_ws;
    a.T = 796; a.fv = (10 << 20) + (52 << 12) + (5 << 8) + 32; a.fp = 506; a.fu = 20; a.fw = 300; a.l1 = 200; a.r0 = 110; a.r1 = 0x11;
    prologue_kernel<<<512, 256, 0, stream>>>((const u32*)d_in[0], (char*)d_ws);
    main_kernel<<<256, 512, 0, stream>>>(a);
}
```
